```python
import torch

def _softmax(x, axis=-1):
    return torch.softmax(x, dim=axis)
def _gelu(x):
    return torch.nn.functional.gelu(x, approximate="tanh")
def _silu(x):
    return torch.nn.functional.silu(x)
def _sigmoid(x):
    return torch.sigmoid(x)
def _layernorm(x, axis=-1, eps=1e-5):
    m = x.mean(dim=axis, keepdim=True)
    v = x.var(dim=axis, keepdim=True, unbiased=False)
    return (x - m) / torch.sqrt(v + eps)
def _scatter_add(tgt, idx, src, axis=0):
    return tgt.clone().index_add_(axis, idx, src)
def _segment_sum(src, seg, n):
    out = torch.zeros((n,) + src.shape[1:], dtype=src.dtype)
    return out.index_add_(0, seg, src)
def _pad(x, pw):
    flat = tuple(v for pair in reversed(pw) for v in pair)
    return torch.nn.functional.pad(x, flat)

kernel_name = 'concat_heads__gqa_block_cached__mha_block_cached__near_ridge'

def setup_inputs():

    _w = (lambda a: (lambda t: t.to(torch.bfloat16) if t.is_floating_point() else t)(torch.as_tensor(a)))
    def _A_setup():
        B, Sq, D = 8, 80, 512
        Hq, Hkv, Sk, Dh = 32, 8, 1024, 128
        assert Hq % Hkv == 0, (Hq, Hkv)
        return {
            "x": _w((lambda *s: torch.randn(*s, dtype=torch.bfloat16) * 0.8)(B, Sq, D)),
            "k_cache": _w((lambda *s: torch.randn(*s, dtype=torch.bfloat16) * 0.8)(B, Hkv, Sk, Dh)),
            "v_cache": _w((lambda *s: torch.randn(*s, dtype=torch.bfloat16) * 0.05)(B, Hkv, Sk, Dh)),
            "wq": _w((lambda *s: torch.randn(*s, dtype=torch.bfloat16) * 0.8)(D, Hq * Dh) * (D ** -0.5)),
            "wo": _w((lambda *s: torch.randn(*s, dtype=torch.bfloat16) * 0.05)(Hq * Dh, D) * ((Hq * Dh) ** -0.5)),
        }
    _A_in = _A_setup()

    def _B_setup():
        B, Sq, D = 8, 80, 512
        H, Sk, Dh = 32, 1024, 128
        return {
            "x": _w((lambda *s: torch.randn(*s, dtype=torch.bfloat16) * 0.8)(B, Sq, D)),
            "k_cache": _w((lambda *s: torch.randn(*s, dtype=torch.bfloat16) * 0.8)(B, H, Sk, Dh)),
            "v_cache": _w((lambda *s: torch.randn(*s, dtype=torch.bfloat16) * 0.05)(B, H, Sk, Dh)),
            "wq": _w((lambda *s: torch.randn(*s, dtype=torch.bfloat16) * 0.8)(D, H * Dh) * (D ** -0.5)),
            "wo": _w((lambda *s: torch.randn(*s, dtype=torch.bfloat16) * 0.05)(H * Dh, D) * ((H * Dh) ** -0.5)),
        }
    _B_in = _B_setup()

    return {
        "A_x": _A_in["x"],
        "A_k_cache": _A_in["k_cache"],
        "A_v_cache": _A_in["v_cache"],
        "A_wq": _A_in["wq"],
        "A_wo": _A_in["wo"],
        "B_x": _B_in["x"],
        "B_k_cache": _B_in["k_cache"],
        "B_v_cache": _B_in["v_cache"],
        "B_wq": _B_in["wq"],
        "B_wo": _B_in["wo"],
    }

def reference(A_x, A_k_cache, A_v_cache, A_wq, A_wo, B_x, B_k_cache, B_v_cache, B_wq, B_wo):
    def _A_ref(x, k_cache, v_cache, wq, wo):
        B, Sq, D = x.shape
        Hq, Dh = 32, 128
        Hkv = k_cache.shape[1]
        rep = Hq // Hkv
        q = (lambda x, *d: x.permute(*d))((x @ wq).reshape(B, Sq, Hq, Dh), 0, 2, 1, 3)
        k = (lambda x, n, axis: torch.repeat_interleave(x, n, dim=axis))(k_cache, rep, axis=1)
        v = (lambda x, n, axis: torch.repeat_interleave(x, n, dim=axis))(v_cache, rep, axis=1)
        scores = torch.einsum("bhqd,bhkd->bhqk", q, k) * (Dh ** -0.5)
        p = _softmax(scores, axis=-1)
        o = torch.einsum("bhqk,bhkd->bhqd", p, v)
        return (lambda x, *d: x.permute(*d))(o, 0, 2, 1, 3).reshape(B, Sq, Hq * Dh) @ wo

    def _B_ref(x, k_cache, v_cache, wq, wo):
        B, Sq, D = x.shape
        H, Dh = 32, 128
        q = (lambda x, *d: x.permute(*d))((x @ wq).reshape(B, Sq, H, Dh), 0, 2, 1, 3)
        scores = torch.einsum("bhqd,bhkd->bhqk", q, k_cache) * (Dh ** -0.5)
        p = _softmax(scores, axis=-1)
        o = torch.einsum("bhqk,bhkd->bhqd", p, v_cache)
        return (lambda x, *d: x.permute(*d))(o, 0, 2, 1, 3).reshape(B, Sq, H * Dh) @ wo

    a = _A_ref(A_x, A_k_cache, A_v_cache, A_wq, A_wo)
    b = _B_ref(B_x, B_k_cache, B_v_cache, B_wq, B_wo)
    return torch.concatenate([a, b], axis=-1)
```

```cpp
#include <hip/hip_runtime.h>
#include <stdint.h>

struct Ptrs { const void* p[2][8]; };

__global__ __launch_bounds__(256, 1) void fused_kernel(Ptrs P) {
  const unsigned w = __builtin_amdgcn_readfirstlane(threadIdx.x >> 6);
  asm volatile(
    "LTOP%=:\n"
    "s_getpc_b64 s[36:37]\n"
    "s_bfe_u32 s34, %1, 0x10003\n"
    "s_lshl_b32 s22, s34, 6\n"
    "s_load_dwordx16 s[4:19], %0, s22\n"
    "v_mbcnt_lo_u32_b32 v0, -1, 0\n"
    "v_mbcnt_hi_u32_b32 v0, -1, v0\n"
    "s_cmp_eq_u32 s34, 0\n"
    "s_cbranch_scc0 LNODLY%=\n"
    "s_and_b32 s37, s37, 0xffff\n"
    "s_mov_b32 s38, LEND%=-LTOP%=-16\n"
    "s_mov_b32 s39, 0x20000\n"
    "v_lshlrev_b32 v116, 4, v0\n"
    "s_lshl_b32 s64, %2, 10\n"
    "LCPF%=:\n"
    "buffer_load_dwordx4 v[244:247], v116, s[36:39], s64 offen\n"
    "s_add_u32 s64, s64, 4096\n"
    "s_cmp_lt_u32 s64, s38\n"
    "s_cbranch_scc1 LCPF%=\n"
    "s_sleep 58\n"
    "LNODLY%=:\n"
    "s_lshr_b32 s20, %1, 5\n"
    "s_and_b32 s22, %1, 7\n"
    "s_bfe_u32 s23, %1, 0x10004\n"
    "s_lshl_b32 s21, s22, 1\n"
    "s_add_u32 s21, s21, s23\n"
    "s_and_b32 s30, %2, 1\n"
    "s_lshr_b32 s31, %2, 1\n"
    "s_lshl_b32 s35, s23, 2\n"
    "s_add_u32 s35, s35, s20\n"
    "s_and_b32 s35, s35, 7\n"
    "s_lshl_b32 s24, s21, 1\n"
    "s_add_u32 s24, s24, s31\n"
    "s_lshl_b32 s25, s20, 5\n"
    "s_add_u32 s25, s25, s24\n"
    "s_lshl_b32 s25, s25, 1\n"
    "s_add_u32 s25, s25, s30\n"
    "s_lshl_b32 s23, s20, 3\n"
    "s_add_u32 s23, s23, s22\n"
    "s_lshl_b32 s23, s23, 1\n"
    "s_add_u32 s23, s23, s30\n"
    "s_add_u32 s22, s23, 9\n"
    "s_cmp_eq_u32 s34, 0\n"
    "s_cselect_b32 s23, s23, s25\n"
    "s_cselect_b32 s69, s22, s25\n"
    "s_and_b32 s69, s69, 15\n"
    "s_lshl_b32 s23, s23, 17\n"
    "s_mul_i32 s25, s20, 81920\n"
    "s_lshl_b32 s24, s24, 8\n"
    "s_lshl_b32 s65, s30, 7\n"
    "s_add_u32 s24, s24, s65\n"
    "s_lshl_b32 s22, s21, 18\n"
    "s_lshl_b32 s65, %2, 8\n"
    "s_add_u32 s22, s22, s65\n"
    "s_mov_b32 s68, 0\n"
    "s_mul_i32 s56, %2, 40960\n"
    "s_mul_i32 s64, %2, 3\n"
    "s_min_u32 s64, s64, 7\n"
    "s_lshl_b32 s59, s64, 13\n"
    "s_lshl_b32 s58, s64, 10\n"
    "v_lshrrev_b32 v208, 3, v0\n"
    "v_and_b32 v209, 7, v0\n"
    "v_xor_b32 v116, v209, v208\n"
    "v_lshlrev_b32 v116, 4, v116\n"
    "v_lshl_add_u32 v110, v208, 10, v116\n"
    "v_and_b32 v116, 2, v208\n"
    "v_xor_b32 v116, v116, v209\n"
    "v_lshlrev_b32 v116, 4, v116\n"
    "v_lshl_add_u32 v108, v208, 13, v116\n"
    "v_xor_b32 v109, 64, v108\n"
    "s_mov_b32 s50, 81920\n"
    "s_mov_b32 s51, 0x20000\n"
    "s_mov_b32 s46, 4186240\n"
    "s_mov_b32 s47, 0x20000\n"
    "s_add_u32 s66, s35, 0\n"
    "s_and_b32 s66, s66, 7\n"
    "s_lshl_b32 s66, s66, 7\n"
    "s_add_u32 s66, s66, s59\n"
    "s_add_u32 m0, s58, 30720\n"
    "s_add_u32 s64, s66, 0\n"
    "s_waitcnt lgkmcnt(0)\n"
    "s_add_u32 s48, s4, s25\n"
    "s_addc_u32 s49, s5, 0\n"
    "s_and_b32 s49, s49, 0xffff\n"
    "s_add_u32 s44, s10, s24\n"
    "s_addc_u32 s45, s11, 0\n"
    "s_and_b32 s45, s45, 0xffff\n"
    "buffer_load_dwordx4 v110, s[48:51], s64 offen offset:0 lds\n"
    "s_add_u32 s65, s66, 7168\n"
    "buffer_load_dwordx4 v110, s[48:51], s65 offen offset:1024 lds\n"
    "s_add_u32 s64, s66, 14336\n"
    "buffer_load_dwordx4 v110, s[48:51], s64 offen offset:2048 lds\n"
    "s_add_u32 s66, s35, 0\n"
    "s_and_b32 s66, s66, 7\n"
    "s_lshl_b32 s66, s66, 19\n"
    "s_add_u32 m0, s56, 8192\n"
    "s_add_u32 s64, s66, 0\n"
    "buffer_load_dwordx4 v108, s[44:47], s64 offen offset:0 lds\n"
    "s_add_u32 s65, s66, 64512\n"
    "buffer_load_dwordx4 v109, s[44:47], s65 offen offset:1024 lds\n"
    "s_add_u32 s64, s66, 129024\n"
    "buffer_load_dwordx4 v108, s[44:47], s64 offen offset:2048 lds\n"
    "s_add_u32 s65, s66, 193536\n"
    "buffer_load_dwordx4 v109, s[44:47], s65 offen offset:3072 lds\n"
    "s_add_u32 m0, s56, 12288\n"
    "s_add_u32 s64, s66, 262144\n"
    "buffer_load_dwordx4 v108, s[44:47], s64 offen offset:0 lds\n"
    "s_add_u32 s65, s66, 326656\n"
    "buffer_load_dwordx4 v109, s[44:47], s65 offen offset:1024 lds\n"
    "s_add_u32 s64, s66, 391168\n"
    "buffer_load_dwordx4 v108, s[44:47], s64 offen offset:2048 lds\n"
    "s_add_u32 s65, s66, 455680\n"
    "buffer_load_dwordx4 v109, s[44:47], s65 offen offset:3072 lds\n"
    "s_add_u32 s6, s6, s23\n"
    "s_addc_u32 s7, s7, 0\n"
    "s_add_u32 s8, s8, s23\n"
    "s_addc_u32 s9, s9, 0\n"
    "s_add_u32 s12, s12, s22\n"
    "s_addc_u32 s13, s13, 0\n"
    "s_xor_b32 s64, %2, 1\n"
    "s_mul_i32 s64, s64, 40960\n"
    "s_add_u32 s57, s68, s64\n"
    "s_add_u32 s28, s14, 67108864\n"
    "s_addc_u32 s29, s15, 0\n"
    "s_load_dwordx2 s[26:27], s[28:29], 0x0\n"
    "s_mov_b32 s36, s6\n"
    "s_and_b32 s37, s7, 0xffff\n"
    "s_mov_b32 s38, 131072\n"
    "s_mov_b32 s39, 0x20000\n"
    "s_mov_b32 s40, s8\n"
    "s_and_b32 s41, s9, 0xffff\n"
    "s_mov_b32 s42, 131072\n"
    "s_mov_b32 s43, 0x20000\n"
    "v_lshrrev_b32 v22, 4, v0\n"
    "v_and_b32 v23, 15, v0\n"
    "v_lshlrev_b32 v116, 1, v22\n"
    "v_xor_b32 v116, v116, v23\n"
    "v_lshlrev_b32 v116, 4, v116\n"
    "v_lshl_add_u32 v7, v22, 8, v116\n"
    "v_xor_b32 v8, 0x80, v7\n"
    "v_xor_b32 v116, v22, v23\n"
    "v_lshlrev_b32 v116, 4, v116\n"
    "v_lshl_add_u32 v3, v22, 8, v116\n"
    "v_xor_b32 v4, 0x40, v3\n"
    "v_xor_b32 v5, 0x80, v3\n"
    "v_xor_b32 v6, 0xc0, v3\n"
    "v_lshrrev_b32 v117, 2, v23\n"
    "v_and_b32 v118, 3, v23\n"
    "v_lshl_add_u32 v119, v117, 3, v118\n"
    "v_lshl_add_u32 v1, v119, 8, v116\n"
    "s_lshl_b32 s64, s30, 7\n"
    "v_xor_b32 v1, s64, v1\n"
    "v_lshl_add_u32 v119, v22, 3, v117\n"
    "v_lshlrev_b32 v2, 8, v119\n"
    "v_and_b32 v120, 1, v22\n"
    "v_lshl_add_u32 v121, v120, 2, v117\n"
    "v_lshl_add_u32 v2, v121, 5, v2\n"
    "v_lshl_add_u32 v2, v118, 3, v2\n"
    "v_xor_b32 v2, s64, v2\n"
    "v_lshrrev_b32 v121, 1, v117\n"
    "v_lshl_add_u32 v121, v120, 1, v121\n"
    "v_lshlrev_b32 v211, 7, v119\n"
    "v_lshl_add_u32 v211, v121, 5, v211\n"
    "v_lshl_add_u32 v211, v118, 3, v211\n"
    "v_add_u32 v13, s56, v211\n"
    "v_xor_b32 v14, 0x20, v13\n"
    "v_xor_b32 v15, 0x40, v13\n"
    "v_xor_b32 v16, 0x60, v13\n"
    "v_and_b32 v121, 7, v23\n"
    "v_xor_b32 v121, v121, v22\n"
    "v_lshlrev_b32 v121, 4, v121\n"
    "v_lshl_add_u32 v210, v23, 7, v121\n"
    "v_lshlrev_b32 v116, 4, v0\n"
    "v_add_u32 v111, s56, v116\n"
    "v_add_u32 v112, s57, v116\n"
    "v_lshrrev_b32 v116, 1, v22\n"
    "v_lshl_add_u32 v116, v116, 4, v23\n"
    "v_lshlrev_b32 v116, 4, v116\n"
    "v_lshl_add_u32 v116, v120, 3, v116\n"
    "v_add_u32 v113, s56, v116\n"
    "v_and_b32 v116, 1, v117\n"
    "v_lshlrev_b32 v211, 10, v116\n"
    "v_lshrrev_b32 v116, 1, v117\n"
    "v_lshl_add_u32 v116, v22, 1, v116\n"
    "v_lshl_add_u32 v211, v116, 7, v211\n"
    "v_lshl_add_u32 v211, v22, 5, v211\n"
    "v_lshl_add_u32 v211, v118, 3, v211\n"
    "v_and_b32 v116, 6, v208\n"
    "v_xor_b32 v116, v116, v209\n"
    "v_lshlrev_b32 v116, 4, v116\n"
    "v_lshl_add_u32 v222, v208, 11, v116\n"
    "v_add_u32 v223, 14336, v222\n"
    "s_mov_b32 s72, 0x3e0293ee\n"
    "s_mov_b32 s73, 0x3e0293ee\n"
    "s_mov_b32 s63, 0x41000000\n"
    "v_mov_b32 v104, 0\n"
    "v_mov_b32 v105, 0\n"
    "v_mov_b32 v106, 0\n"
    "v_mov_b32 v107, 0\n"
    "s_cmp_eq_u32 s34, 0\n"
    "s_cselect_b32 s64, s31, 0\n"
    "s_cmp_eq_u32 s64, 0\n"
    "s_cbranch_scc1 LNOSWAP%=\n"
    "s_mov_b32 s36, s40\n"
    "s_mov_b32 s37, s41\n"
    "s_mov_b32 s38, s42\n"
    "s_mov_b32 s39, s43\n"
    "v_mov_b32 v3, v7\n"
    "v_mov_b32 v4, v8\n"
    "v_mov_b32 v5, v7\n"
    "v_mov_b32 v6, v8\n"
    "LNOSWAP%=:\n"
    "v_mfma_f32_32x32x16_bf16 a[0:15], v[104:107], v[104:107], 0\n"
    "v_mfma_f32_32x32x16_bf16 a[16:31], v[104:107], v[104:107], 0\n"
    "v_mfma_f32_32x32x16_bf16 a[32:47], v[104:107], v[104:107], 0\n"
    "v_mfma_f32_32x32x16_bf16 a[48:63], v[104:107], v[104:107], 0\n"
    "v_mfma_f32_32x32x16_bf16 a[64:79], v[104:107], v[104:107], 0\n"
    "v_mfma_f32_32x32x16_bf16 a[224:239], v[104:107], v[104:107], 0\n"
    "v_mfma_f32_32x32x16_bf16 a[240:255], v[104:107], v[104:107], 0\n"
    "v_mov_b32 v24, 0\n"
    "v_mov_b32 v25, 0\n"
    "v_mov_b32 v26, 0\n"
    "v_mov_b32 v27, 0\n"
    "v_mov_b32 v28, 0\n"
    "v_mov_b32 v29, 0\n"
    "v_mov_b32 v30, 0\n"
    "v_mov_b32 v31, 0\n"
    "v_mov_b32 v32, 0\n"
    "v_mov_b32 v33, 0\n"
    "v_mov_b32 v34, 0\n"
    "v_mov_b32 v35, 0\n"
    "v_mov_b32 v36, 0\n"
    "v_mov_b32 v37, 0\n"
    "v_mov_b32 v38, 0\n"
    "v_mov_b32 v39, 0\n"
    "v_mov_b32 v40, 0\n"
    "v_mov_b32 v41, 0\n"
    "v_mov_b32 v42, 0\n"
    "v_mov_b32 v43, 0\n"
    "s_lshl_b32 s66, %1, 3\n"
    "s_add_u32 s66, s66, 67112960\n"
    "s_add_u32 s52, s14, s66\n"
    "s_addc_u32 s53, s15, 0\n"
    "s_and_b32 s53, s53, 0xffff\n"
    "s_mov_b32 s54, 8\n"
    "s_mov_b32 s55, 0x20000\n"
    "v_lshlrev_b32 v46, 3, v0\n"
    "s_add_u32 s66, s35, 1\n"
    "s_and_b32 s66, s66, 7\n"
    "s_lshl_b32 s66, s66, 7\n"
    "s_add_u32 s66, s66, s59\n"
    "s_add_u32 m0, s58, 71680\n"
    "s_add_u32 s64, s66, 0\n"
    "buffer_load_dwordx4 v110, s[48:51], s64 offen offset:0 lds\n"
    "s_add_u32 s65, s66, 7168\n"
    "buffer_load_dwordx4 v110, s[48:51], s65 offen offset:1024 lds\n"
    "s_add_u32 s64, s66, 14336\n"
    "buffer_load_dwordx4 v110, s[48:51], s64 offen offset:2048 lds\n"
    "s_add_u32 s66, s35, 2\n"
    "s_and_b32 s66, s66, 7\n"
    "s_lshl_b32 s66, s66, 7\n"
    "s_add_u32 s66, s66, s59\n"
    "s_add_u32 m0, s58, 112640\n"
    "s_add_u32 s64, s66, 0\n"
    "buffer_load_dwordx4 v110, s[48:51], s64 offen offset:0 lds\n"
    "s_add_u32 s65, s66, 7168\n"
    "buffer_load_dwordx4 v110, s[48:51], s65 offen offset:1024 lds\n"
    "s_add_u32 s64, s66, 14336\n"
    "buffer_load_dwordx4 v110, s[48:51], s64 offen offset:2048 lds\n"
    "s_add_u32 s66, s35, 3\n"
    "s_and_b32 s66, s66, 7\n"
    "s_lshl_b32 s66, s66, 7\n"
    "s_add_u32 s66, s66, s59\n"
    "s_add_u32 m0, s58, 153600\n"
    "s_add_u32 s64, s66, 0\n"
    "buffer_load_dwordx4 v110, s[48:51], s64 offen offset:0 lds\n"
    "s_add_u32 s65, s66, 7168\n"
    "buffer_load_dwordx4 v110, s[48:51], s65 offen offset:1024 lds\n"
    "s_add_u32 s64, s66, 14336\n"
    "buffer_load_dwordx4 v110, s[48:51], s64 offen offset:2048 lds\n"
    "s_add_u32 s66, s35, 1\n"
    "s_and_b32 s66, s66, 7\n"
    "s_lshl_b32 s66, s66, 19\n"
    "s_add_u32 m0, s56, 16384\n"
    "s_add_u32 s64, s66, 0\n"
    "buffer_load_dwordx4 v108, s[44:47], s64 offen offset:0 lds\n"
    "s_add_u32 s65, s66, 64512\n"
    "buffer_load_dwordx4 v109, s[44:47], s65 offen offset:1024 lds\n"
    "s_add_u32 s64, s66, 129024\n"
    "buffer_load_dwordx4 v108, s[44:47], s64 offen offset:2048 lds\n"
    "s_add_u32 s65, s66, 193536\n"
    "buffer_load_dwordx4 v109, s[44:47], s65 offen offset:3072 lds\n"
    "s_add_u32 m0, s56, 20480\n"
    "s_add_u32 s64, s66, 262144\n"
    "buffer_load_dwordx4 v108, s[44:47], s64 offen offset:0 lds\n"
    "s_add_u32 s65, s66, 326656\n"
    "buffer_load_dwordx4 v109, s[44:47], s65 offen offset:1024 lds\n"
    "s_add_u32 s64, s66, 391168\n"
    "buffer_load_dwordx4 v108, s[44:47], s64 offen offset:2048 lds\n"
    "s_add_u32 s65, s66, 455680\n"
    "buffer_load_dwordx4 v109, s[44:47], s65 offen offset:3072 lds\n"
    "s_waitcnt vmcnt(17)\n"
    "s_barrier\n"
    "s_add_u32 s67, s68, 30720\n"
    "v_add_u32 v114, s67, v210\n"
    "v_xor_b32 v115, 64, v114\n"
    "ds_read_b128 a[80:83], v114 offset:0\n"
    "ds_read_b128 a[84:87], v114 offset:2048\n"
    "ds_read_b128 a[88:91], v114 offset:4096\n"
    "ds_read_b128 a[92:95], v114 offset:6144\n"
    "ds_read_b128 a[96:99], v114 offset:8192\n"
    "ds_read_b64_tr_b16 a[160:161], v13 offset:8192\n"
    "ds_read_b64_tr_b16 a[162:163], v13 offset:8704\n"
    "ds_read_b64_tr_b16 a[164:165], v14 offset:8192\n"
    "ds_read_b64_tr_b16 a[166:167], v14 offset:8704\n"
    "ds_read_b64_tr_b16 a[168:169], v15 offset:8192\n"
    "ds_read_b64_tr_b16 a[170:171], v15 offset:8704\n"
    "ds_read_b64_tr_b16 a[172:173], v16 offset:8192\n"
    "ds_read_b64_tr_b16 a[174:175], v16 offset:8704\n"
    "ds_read_b128 a[100:103], v115 offset:0\n"
    "ds_read_b128 a[104:107], v115 offset:2048\n"
    "ds_read_b128 a[108:111], v115 offset:4096\n"
    "ds_read_b128 a[112:115], v115 offset:6144\n"
    "ds_read_b128 a[116:119], v115 offset:8192\n"
    "ds_read_b64_tr_b16 a[176:177], v13 offset:12288\n"
    "ds_read_b64_tr_b16 a[178:179], v13 offset:12800\n"
    "ds_read_b64_tr_b16 a[180:181], v14 offset:12288\n"
    "ds_read_b64_tr_b16 a[182:183], v14 offset:12800\n"
    "ds_read_b64_tr_b16 a[184:185], v15 offset:12288\n"
    "ds_read_b64_tr_b16 a[186:187], v15 offset:12800\n"
    "ds_read_b64_tr_b16 a[188:189], v16 offset:12288\n"
    "ds_read_b64_tr_b16 a[190:191], v16 offset:12800\n"
    "s_add_u32 s66, s35, 2\n"
    "s_and_b32 s66, s66, 7\n"
    "s_lshl_b32 s66, s66, 19\n"
    "s_add_u32 m0, s56, 0\n"
    "s_add_u32 s64, s66, 0\n"
    "buffer_load_dwordx4 v108, s[44:47], s64 offen offset:0 lds\n"
    "s_add_u32 s65, s66, 64512\n"
    "buffer_load_dwordx4 v109, s[44:47], s65 offen offset:1024 lds\n"
    "s_add_u32 s64, s66, 129024\n"
    "buffer_load_dwordx4 v108, s[44:47], s64 offen offset:2048 lds\n"
    "s_add_u32 s65, s66, 193536\n"
    "buffer_load_dwordx4 v109, s[44:47], s65 offen offset:3072 lds\n"
    "s_add_u32 m0, s56, 4096\n"
    "s_add_u32 s64, s66, 262144\n"
    "buffer_load_dwordx4 v108, s[44:47], s64 offen offset:0 lds\n"
    "s_add_u32 s65, s66, 326656\n"
    "buffer_load_dwordx4 v109, s[44:47], s65 offen offset:1024 lds\n"
    "s_add_u32 s64, s66, 391168\n"
    "buffer_load_dwordx4 v108, s[44:47], s64 offen offset:2048 lds\n"
    "s_add_u32 s65, s66, 455680\n"
    "buffer_load_dwordx4 v109, s[44:47], s65 offen offset:3072 lds\n"
    "s_waitcnt vmcnt(8) lgkmcnt(0)\n"
    "s_barrier\n"
    "v_mfma_f32_16x16x32_bf16 v[128:131], a[160:163], a[80:83], 0\n"
    "s_add_u32 s66, s35, 3\n"
    "s_and_b32 s66, s66, 7\n"
    "s_lshl_b32 s66, s66, 19\n"
    "v_mfma_f32_16x16x32_bf16 v[132:135], a[160:163], a[84:87], 0\n"
    "s_add_u32 m0, s56, 8192\n"
    "s_add_u32 s64, s66, 0\n"
    "s_add_u32 s67, s68, 71680\n"
    "v_mfma_f32_16x16x32_bf16 v[136:139], a[160:163], a[88:91], 0\n"
    "buffer_load_dwordx4 v108, s[44:47], s64 offen offset:0 lds\n"
    "v_add_u32 v114, s67, v210\n"
    "v_mfma_f32_16x16x32_bf16 v[140:143], a[160:163], a[92:95], 0\n"
    "ds_read_b128 a[120:123], v114 offset:0\n"
    "v_xor_b32 v115, 64, v114\n"
    "v_mfma_f32_16x16x32_bf16 v[144:147], a[160:163], a[96:99], 0\n"
    "ds_read_b128 a[124:127], v114 offset:2048\n"
    "s_add_u32 s65, s66, 64512\n"
    "v_mfma_f32_16x16x32_bf16 v[148:151], a[164:167], a[80:83], 0\n"
    "buffer_load_dwordx4 v109, s[44:47], s65 offen offset:1024 lds\n"
    "s_add_u32 s64, s66, 129024\n"
    "v_mfma_f32_16x16x32_bf16 v[152:155], a[164:167], a[84:87], 0\n"
    "ds_read_b128 a[128:131], v114 offset:4096\n"
    "s_add_u32 s65, s66, 193536\n"
    "v_mfma_f32_16x16x32_bf16 v[156:159], a[164:167], a[88:91], 0\n"
    "buffer_load_dwordx4 v108, s[44:47], s64 offen offset:2048 lds\n"
    "s_add_u32 s64, s66, 262144\n"
    "v_mfma_f32_16x16x32_bf16 v[160:163], a[164:167], a[92:95], 0\n"
    "ds_read_b128 a[132:135], v114 offset:6144\n"
    "v_mfma_f32_16x16x32_bf16 v[164:167], a[164:167], a[96:99], 0\n"
    "ds_read_b128 a[136:139], v114 offset:8192\n"
    "v_mfma_f32_16x16x32_bf16 v[168:171], a[168:171], a[80:83], 0\n"
    "ds_read_b64_tr_b16 a[192:193], v13 offset:16384\n"
    "v_mfma_f32_16x16x32_bf16 v[172:175], a[168:171], a[84:87], 0\n"
    "buffer_load_dwordx4 v109, s[44:47], s65 offen offset:3072 lds\n"
    "s_add_u32 m0, s56, 12288\n"
    "v_mfma_f32_16x16x32_bf16 v[176:179], a[168:171], a[88:91], 0\n"
    "ds_read_b64_tr_b16 a[194:195], v13 offset:16896\n"
    "s_add_u32 s65, s66, 326656\n"
    "v_mfma_f32_16x16x32_bf16 v[180:183], a[168:171], a[92:95], 0\n"
    "ds_read_b64_tr_b16 a[196:197], v14 offset:16384\n"
    "v_mfma_f32_16x16x32_bf16 v[184:187], a[168:171], a[96:99], 0\n"
    "ds_read_b64_tr_b16 a[198:199], v14 offset:16896\n"
    "v_mfma_f32_16x16x32_bf16 v[188:191], a[172:175], a[80:83], 0\n"
    "buffer_load_dwordx4 v108, s[44:47], s64 offen offset:0 lds\n"
    "s_add_u32 s64, s66, 391168\n"
    "v_mfma_f32_16x16x32_bf16 v[192:195], a[172:175], a[84:87], 0\n"
    "ds_read_b64_tr_b16 a[200:201], v15 offset:16384\n"
    "v_mfma_f32_16x16x32_bf16 v[196:199], a[172:175], a[88:91], 0\n"
    "ds_read_b64_tr_b16 a[202:203], v15 offset:16896\n"
    "v_mfma_f32_16x16x32_bf16 v[200:203], a[172:175], a[92:95], 0\n"
    "ds_read_b64_tr_b16 a[204:205], v16 offset:16384\n"
    "v_mfma_f32_16x16x32_bf16 v[204:207], a[172:175], a[96:99], 0\n"
    "ds_read_b64_tr_b16 a[206:207], v16 offset:16896\n"
    "v_mfma_f32_16x16x32_bf16 v[128:131], a[176:179], a[100:103], v[128:131]\n"
    "buffer_load_dwordx4 v109, s[44:47], s65 offen offset:1024 lds\n"
    "s_add_u32 s65, s66, 455680\n"
    "v_mfma_f32_16x16x32_bf16 v[132:135], a[176:179], a[104:107], v[132:135]\n"
    "ds_read_b128 a[140:143], v115 offset:0\n"
    "s_add_u32 s66, s35, 4\n"
    "v_mfma_f32_16x16x32_bf16 v[136:139], a[176:179], a[108:111], v[136:139]\n"
    "ds_read_b128 a[144:147], v115 offset:2048\n"
    "s_and_b32 s66, s66, 7\n"
    "v_mfma_f32_16x16x32_bf16 v[140:143], a[176:179], a[112:115], v[140:143]\n"
    "buffer_load_dwordx4 v108, s[44:47], s64 offen offset:2048 lds\n"
    "s_lshl_b32 s66, s66, 7\n"
    "v_mfma_f32_16x16x32_bf16 v[144:147], a[176:179], a[116:119], v[144:147]\n"
    "ds_read_b128 a[148:151], v115 offset:4096\n"
    "s_add_u32 s66, s66, s59\n"
    "v_mfma_f32_16x16x32_bf16 v[148:151], a[180:183], a[100:103], v[148:151]\n"
    "ds_read_b128 a[152:155], v115 offset:6144\n"
    "s_add_u32 s64, s66, 0\n"
    "v_mfma_f32_16x16x32_bf16 v[152:155], a[180:183], a[104:107], v[152:155]\n"
    "ds_read_b128 a[156:159], v115 offset:8192\n"
    "v_mfma_f32_16x16x32_bf16 v[156:159], a[180:183], a[108:111], v[156:159]\n"
    "ds_read_b64_tr_b16 a[208:209], v13 offset:20480\n"
    "v_mfma_f32_16x16x32_bf16 v[160:163], a[180:183], a[112:115], v[160:163]\n"
    "buffer_load_dwordx4 v109, s[44:47], s65 offen offset:3072 lds\n"
    "s_add_u32 m0, s58, 30720\n"
    "v_mfma_f32_16x16x32_bf16 v[164:167], a[180:183], a[116:119], v[164:167]\n"
    "ds_read_b64_tr_b16 a[210:211], v13 offset:20992\n"
    "s_add_u32 s65, s66, 7168\n"
    "v_mfma_f32_16x16x32_bf16 v[168:171], a[184:187], a[100:103], v[168:171]\n"
    "ds_read_b64_tr_b16 a[212:213], v14 offset:20480\n"
    "v_mfma_f32_16x16x32_bf16 v[172:175], a[184:187], a[104:107], v[172:175]\n"
    "ds_read_b64_tr_b16 a[214:215], v14 offset:20992\n"
    "v_mfma_f32_16x16x32_bf16 v[176:179], a[184:187], a[108:111], v[176:179]\n"
    "buffer_load_dwordx4 v110, s[48:51], s64 offen offset:0 lds\n"
    "s_add_u32 s64, s66, 14336\n"
    "v_mfma_f32_16x16x32_bf16 v[180:183], a[184:187], a[112:115], v[180:183]\n"
    "ds_read_b64_tr_b16 a[216:217], v15 offset:20480\n"
    "v_mfma_f32_16x16x32_bf16 v[184:187], a[184:187], a[116:119], v[184:187]\n"
    "ds_read_b64_tr_b16 a[218:219], v15 offset:20992\n"
    "v_mfma_f32_16x16x32_bf16 v[188:191], a[188:191], a[100:103], v[188:191]\n"
    "ds_read_b64_tr_b16 a[220:221], v16 offset:20480\n"
    "v_mfma_f32_16x16x32_bf16 v[192:195], a[188:191], a[104:107], v[192:195]\n"
    "buffer_load_dwordx4 v110, s[48:51], s65 offen offset:1024 lds\n"
    "ds_read_b64_tr_b16 a[222:223], v16 offset:20992\n"
    "v_mfma_f32_16x16x32_bf16 v[196:199], a[188:191], a[108:111], v[196:199]\n"
    "buffer_load_dwordx4 v110, s[48:51], s64 offen offset:2048 lds\n"
    "v_mfma_f32_16x16x32_bf16 v[200:203], a[188:191], a[112:115], v[200:203]\n"
    "v_mfma_f32_16x16x32_bf16 v[204:207], a[188:191], a[116:119], v[204:207]\n"
    "s_waitcnt vmcnt(11) lgkmcnt(0)\n"
    "s_barrier\n"
    "v_mfma_f32_16x16x32_bf16 v[128:131], a[192:195], a[120:123], v[128:131]\n"
    "s_add_u32 s66, s35, 4\n"
    "s_and_b32 s66, s66, 7\n"
    "s_lshl_b32 s66, s66, 19\n"
    "v_mfma_f32_16x16x32_bf16 v[132:135], a[192:195], a[124:127], v[132:135]\n"
    "s_add_u32 m0, s56, 16384\n"
    "s_add_u32 s64, s66, 0\n"
    "s_add_u32 s67, s68, 112640\n"
    "v_mfma_f32_16x16x32_bf16 v[136:139], a[192:195], a[128:131], v[136:139]\n"
    "buffer_load_dwordx4 v108, s[44:47], s64 offen offset:0 lds\n"
    "v_add_u32 v114, s67, v210\n"
    "v_mfma_f32_16x16x32_bf16 v[140:143], a[192:195], a[132:135], v[140:143]\n"
    "ds_read_b128 a[80:83], v114 offset:0\n"
    "v_xor_b32 v115, 64, v114\n"
    "v_mfma_f32_16x16x32_bf16 v[144:147], a[192:195], a[136:139], v[144:147]\n"
    "ds_read_b128 a[84:87], v114 offset:2048\n"
    "s_add_u32 s65, s66, 64512\n"
    "v_mfma_f32_16x16x32_bf16 v[148:151], a[196:199], a[120:123], v[148:151]\n"
    "buffer_load_dwordx4 v109, s[44:47], s65 offen offset:1024 lds\n"
    "s_add_u32 s64, s66, 129024\n"
    "v_mfma_f32_16x16x32_bf16 v[152:155], a[196:199], a[124:127], v[152:155]\n"
    "ds_read_b128 a[88:91], v114 offset:4096\n"
    "s_add_u32 s65, s66, 193536\n"
    "v_mfma_f32_16x16x32_bf16 v[156:159], a[196:199], a[128:131], v[156:159]\n"
    "buffer_load_dwordx4 v108, s[44:47], s64 offen offset:2048 lds\n"
    "s_add_u32 s64, s66, 262144\n"
    "v_mfma_f32_16x16x32_bf16 v[160:163], a[196:199], a[132:135], v[160:163]\n"
    "ds_read_b128 a[92:95], v114 offset:6144\n"
    "v_mfma_f32_16x16x32_bf16 v[164:167], a[196:199], a[136:139], v[164:167]\n"
    "ds_read_b128 a[96:99], v114 offset:8192\n"
    "v_mfma_f32_16x16x32_bf16 v[168:171], a[200:203], a[120:123], v[168:171]\n"
    "ds_read_b64_tr_b16 a[160:161], v13 offset:0\n"
    "v_mfma_f32_16x16x32_bf16 v[172:175], a[200:203], a[124:127], v[172:175]\n"
    "buffer_load_dwordx4 v109, s[44:47], s65 offen offset:3072 lds\n"
    "s_add_u32 m0, s56, 20480\n"
    "v_mfma_f32_16x16x32_bf16 v[176:179], a[200:203], a[128:131], v[176:179]\n"
    "ds_read_b64_tr_b16 a[162:163], v13 offset:512\n"
    "s_add_u32 s65, s66, 326656\n"
    "v_mfma_f32_16x16x32_bf16 v[180:183], a[200:203], a[132:135], v[180:183]\n"
    "ds_read_b64_tr_b16 a[164:165], v14 offset:0\n"
    "v_mfma_f32_16x16x32_bf16 v[184:187], a[200:203], a[136:139], v[184:187]\n"
    "ds_read_b64_tr_b16 a[166:167], v14 offset:512\n"
    "v_mfma_f32_16x16x32_bf16 v[188:191], a[204:207], a[120:123], v[188:191]\n"
    "buffer_load_dwordx4 v108, s[44:47], s64 offen offset:0 lds\n"
    "s_add_u32 s64, s66, 391168\n"
    "v_mfma_f32_16x16x32_bf16 v[192:195], a[204:207], a[124:127], v[192:195]\n"
    "ds_read_b64_tr_b16 a[168:169], v15 offset:0\n"
    "v_mfma_f32_16x16x32_bf16 v[196:199], a[204:207], a[128:131], v[196:199]\n"
    "ds_read_b64_tr_b16 a[170:171], v15 offset:512\n"
    "v_mfma_f32_16x16x32_bf16 v[200:203], a[204:207], a[132:135], v[200:203]\n"
    "ds_read_b64_tr_b16 a[172:173], v16 offset:0\n"
    "v_mfma_f32_16x16x32_bf16 v[204:207], a[204:207], a[136:139], v[204:207]\n"
    "ds_read_b64_tr_b16 a[174:175], v16 offset:512\n"
    "v_mfma_f32_16x16x32_bf16 v[128:131], a[208:211], a[140:143], v[128:131]\n"
    "buffer_load_dwordx4 v109, s[44:47], s65 offen offset:1024 lds\n"
    "s_add_u32 s65, s66, 455680\n"
    "v_mfma_f32_16x16x32_bf16 v[132:135], a[208:211], a[144:147], v[132:135]\n"
    "ds_read_b128 a[100:103], v115 offset:0\n"
    "s_add_u32 s66, s35, 5\n"
    "v_mfma_f32_16x16x32_bf16 v[136:139], a[208:211], a[148:151], v[136:139]\n"
    "ds_read_b128 a[104:107], v115 offset:2048\n"
    "s_and_b32 s66, s66, 7\n"
    "v_mfma_f32_16x16x32_bf16 v[140:143], a[208:211], a[152:155], v[140:143]\n"
    "buffer_load_dwordx4 v108, s[44:47], s64 offen offset:2048 lds\n"
    "s_lshl_b32 s66, s66, 7\n"
    "v_mfma_f32_16x16x32_bf16 v[144:147], a[208:211], a[156:159], v[144:147]\n"
    "ds_read_b128 a[108:111], v115 offset:4096\n"
    "s_add_u32 s66, s66, s59\n"
    "v_mfma_f32_16x16x32_bf16 v[148:151], a[212:215], a[140:143], v[148:151]\n"
    "ds_read_b128 a[112:115], v115 offset:6144\n"
    "s_add_u32 s64, s66, 0\n"
    "v_mfma_f32_16x16x32_bf16 v[152:155], a[212:215], a[144:147], v[152:155]\n"
    "ds_read_b128 a[116:119], v115 offset:8192\n"
    "v_mfma_f32_16x16x32_bf16 v[156:159], a[212:215], a[148:151], v[156:159]\n"
    "ds_read_b64_tr_b16 a[176:177], v13 offset:4096\n"
    "v_mfma_f32_16x16x32_bf16 v[160:163], a[212:215], a[152:155], v[160:163]\n"
    "buffer_load_dwordx4 v109, s[44:47], s65 offen offset:3072 lds\n"
    "s_add_u32 m0, s58, 71680\n"
    "v_mfma_f32_16x16x32_bf16 v[164:167], a[212:215], a[156:159], v[164:167]\n"
    "ds_read_b64_tr_b16 a[178:179], v13 offset:4608\n"
    "s_add_u32 s65, s66, 7168\n"
    "v_mfma_f32_16x16x32_bf16 v[168:171], a[216:219], a[140:143], v[168:171]\n"
    "ds_read_b64_tr_b16 a[180:181], v14 offset:4096\n"
    "v_mfma_f32_16x16x32_bf16 v[172:175], a[216:219], a[144:147], v[172:175]\n"
    "ds_read_b64_tr_b16 a[182:183], v14 offset:4608\n"
    "v_mfma_f32_16x16x32_bf16 v[176:179], a[216:219], a[148:151], v[176:179]\n"
    "buffer_load_dwordx4 v110, s[48:51], s64 offen offset:0 lds\n"
    "s_add_u32 s64, s66, 14336\n"
    "v_mfma_f32_16x16x32_bf16 v[180:183], a[216:219], a[152:155], v[180:183]\n"
    "ds_read_b64_tr_b16 a[184:185], v15 offset:4096\n"
    "v_mfma_f32_16x16x32_bf16 v[184:187], a[216:219], a[156:159], v[184:187]\n"
    "ds_read_b64_tr_b16 a[186:187], v15 offset:4608\n"
    "v_mfma_f32_16x16x32_bf16 v[188:191], a[220:223], a[140:143], v[188:191]\n"
    "ds_read_b64_tr_b16 a[188:189], v16 offset:4096\n"
    "v_mfma_f32_16x16x32_bf16 v[192:195], a[220:223], a[144:147], v[192:195]\n"
    "buffer_load_dwordx4 v110, s[48:51], s65 offen offset:1024 lds\n"
    "ds_read_b64_tr_b16 a[190:191], v16 offset:4608\n"
    "v_mfma_f32_16x16x32_bf16 v[196:199], a[220:223], a[148:151], v[196:199]\n"
    "buffer_load_dwordx4 v110, s[48:51], s64 offen offset:2048 lds\n"
    "v_mfma_f32_16x16x32_bf16 v[200:203], a[220:223], a[152:155], v[200:203]\n"
    "v_mfma_f32_16x16x32_bf16 v[204:207], a[220:223], a[156:159], v[204:207]\n"
    "s_waitcnt vmcnt(14) lgkmcnt(0)\n"
    "s_barrier\n"
    "v_mfma_f32_16x16x32_bf16 v[128:131], a[160:163], a[80:83], v[128:131]\n"
    "s_add_u32 s66, s35, 5\n"
    "s_and_b32 s66, s66, 7\n"
    "s_lshl_b32 s66, s66, 19\n"
    "v_mfma_f32_16x16x32_bf16 v[132:135], a[160:163], a[84:87], v[132:135]\n"
    "s_add_u32 m0, s56, 0\n"
    "s_add_u32 s64, s66, 0\n"
    "s_add_u32 s67, s68, 153600\n"
    "v_mfma_f32_16x16x32_bf16 v[136:139], a[160:163], a[88:91], v[136:139]\n"
    "buffer_load_dwordx4 v108, s[44:47], s64 offen offset:0 lds\n"
    "v_add_u32 v114, s67, v210\n"
    "v_mfma_f32_16x16x32_bf16 v[140:143], a[160:163], a[92:95], v[140:143]\n"
    "ds_read_b128 a[120:123], v114 offset:0\n"
    "v_xor_b32 v115, 64, v114\n"
    "v_mfma_f32_16x16x32_bf16 v[144:147], a[160:163], a[96:99], v[144:147]\n"
    "ds_read_b128 a[124:127], v114 offset:2048\n"
    "s_add_u32 s65, s66, 64512\n"
    "v_mfma_f32_16x16x32_bf16 v[148:151], a[164:167], a[80:83], v[148:151]\n"
    "buffer_load_dwordx4 v109, s[44:47], s65 offen offset:1024 lds\n"
    "s_add_u32 s64, s66, 129024\n"
    "v_mfma_f32_16x16x32_bf16 v[152:155], a[164:167], a[84:87], v[152:155]\n"
    "ds_read_b128 a[128:131], v114 offset:4096\n"
    "s_add_u32 s65, s66, 193536\n"
    "v_mfma_f32_16x16x32_bf16 v[156:159], a[164:167], a[88:91], v[156:159]\n"
    "buffer_load_dwordx4 v108, s[44:47], s64 offen offset:2048 lds\n"
    "s_add_u32 s64, s66, 262144\n"
    "v_mfma_f32_16x16x32_bf16 v[160:163], a[164:167], a[92:95], v[160:163]\n"
    "ds_read_b128 a[132:135], v114 offset:6144\n"
    "v_mfma_f32_16x16x32_bf16 v[164:167], a[164:167], a[96:99], v[164:167]\n"
    "ds_read_b128 a[136:139], v114 offset:8192\n"
    "v_mfma_f32_16x16x32_bf16 v[168:171], a[168:171], a[80:83], v[168:171]\n"
    "ds_read_b64_tr_b16 a[192:193], v13 offset:8192\n"
    "v_mfma_f32_16x16x32_bf16 v[172:175], a[168:171], a[84:87], v[172:175]\n"
    "buffer_load_dwordx4 v109, s[44:47], s65 offen offset:3072 lds\n"
    "s_add_u32 m0, s56, 4096\n"
    "v_mfma_f32_16x16x32_bf16 v[176:179], a[168:171], a[88:91], v[176:179]\n"
    "ds_read_b64_tr_b16 a[194:195], v13 offset:8704\n"
    "s_add_u32 s65, s66, 326656\n"
    "v_mfma_f32_16x16x32_bf16 v[180:183], a[168:171], a[92:95], v[180:183]\n"
    "ds_read_b64_tr_b16 a[196:197], v14 offset:8192\n"
    "v_mfma_f32_16x16x32_bf16 v[184:187], a[168:171], a[96:99], v[184:187]\n"
    "ds_read_b64_tr_b16 a[198:199], v14 offset:8704\n"
    "v_mfma_f32_16x16x32_bf16 v[188:191], a[172:175], a[80:83], v[188:191]\n"
    "buffer_load_dwordx4 v108, s[44:47], s64 offen offset:0 lds\n"
    "s_add_u32 s64, s66, 391168\n"
    "v_mfma_f32_16x16x32_bf16 v[192:195], a[172:175], a[84:87], v[192:195]\n"
    "ds_read_b64_tr_b16 a[200:201], v15 offset:8192\n"
    "v_mfma_f32_16x16x32_bf16 v[196:199], a[172:175], a[88:91], v[196:199]\n"
    "ds_read_b64_tr_b16 a[202:203], v15 offset:8704\n"
    "v_mfma_f32_16x16x32_bf16 v[200:203], a[172:175], a[92:95], v[200:203]\n"
    "ds_read_b64_tr_b16 a[204:205], v16 offset:8192\n"
    "v_mfma_f32_16x16x32_bf16 v[204:207], a[172:175], a[96:99], v[204:207]\n"
    "ds_read_b64_tr_b16 a[206:207], v16 offset:8704\n"
    "v_mfma_f32_16x16x32_bf16 v[128:131], a[176:179], a[100:103], v[128:131]\n"
    "buffer_load_dwordx4 v109, s[44:47], s65 offen offset:1024 lds\n"
    "s_add_u32 s65, s66, 455680\n"
    "v_mfma_f32_16x16x32_bf16 v[132:135], a[176:179], a[104:107], v[132:135]\n"
    "ds_read_b128 a[140:143], v115 offset:0\n"
    "s_add_u32 s66, s35, 6\n"
    "v_mfma_f32_16x16x32_bf16 v[136:139], a[176:179], a[108:111], v[136:139]\n"
    "ds_read_b128 a[144:147], v115 offset:2048\n"
    "s_and_b32 s66, s66, 7\n"
    "v_mfma_f32_16x16x32_bf16 v[140:143], a[176:179], a[112:115], v[140:143]\n"
    "buffer_load_dwordx4 v108, s[44:47], s64 offen offset:2048 lds\n"
    "s_lshl_b32 s66, s66, 7\n"
    "v_mfma_f32_16x16x32_bf16 v[144:147], a[176:179], a[116:119], v[144:147]\n"
    "ds_read_b128 a[148:151], v115 offset:4096\n"
    "s_add_u32 s66, s66, s59\n"
    "v_mfma_f32_16x16x32_bf16 v[148:151], a[180:183], a[100:103], v[148:151]\n"
    "ds_read_b128 a[152:155], v115 offset:6144\n"
    "s_add_u32 s64, s66, 0\n"
    "v_mfma_f32_16x16x32_bf16 v[152:155], a[180:183], a[104:107], v[152:155]\n"
    "ds_read_b128 a[156:159], v115 offset:8192\n"
    "v_mfma_f32_16x16x32_bf16 v[156:159], a[180:183], a[108:111], v[156:159]\n"
    "ds_read_b64_tr_b16 a[208:209], v13 offset:12288\n"
    "v_mfma_f32_16x16x32_bf16 v[160:163], a[180:183], a[112:115], v[160:163]\n"
    "buffer_load_dwordx4 v109, s[44:47], s65 offen offset:3072 lds\n"
    "s_add_u32 m0, s58, 112640\n"
    "v_mfma_f32_16x16x32_bf16 v[164:167], a[180:183], a[116:119], v[164:167]\n"
    "ds_read_b64_tr_b16 a[210:211], v13 offset:12800\n"
    "s_add_u32 s65, s66, 7168\n"
    "v_mfma_f32_16x16x32_bf16 v[168:171], a[184:187], a[100:103], v[168:171]\n"
    "ds_read_b64_tr_b16 a[212:213], v14 offset:12288\n"
    "v_mfma_f32_16x16x32_bf16 v[172:175], a[184:187], a[104:107], v[172:175]\n"
    "ds_read_b64_tr_b16 a[214:215], v14 offset:12800\n"
    "v_mfma_f32_16x16x32_bf16 v[176:179], a[184:187], a[108:111], v[176:179]\n"
    "buffer_load_dwordx4 v110, s[48:51], s64 offen offset:0 lds\n"
    "s_add_u32 s64, s66, 14336\n"
    "v_mfma_f32_16x16x32_bf16 v[180:183], a[184:187], a[112:115], v[180:183]\n"
    "ds_read_b64_tr_b16 a[216:217], v15 offset:12288\n"
    "v_mfma_f32_16x16x32_bf16 v[184:187], a[184:187], a[116:119], v[184:187]\n"
    "ds_read_b64_tr_b16 a[218:219], v15 offset:12800\n"
    "v_mfma_f32_16x16x32_bf16 v[188:191], a[188:191], a[100:103], v[188:191]\n"
    "ds_read_b64_tr_b16 a[220:221], v16 offset:12288\n"
    "v_mfma_f32_16x16x32_bf16 v[192:195], a[188:191], a[104:107], v[192:195]\n"
    "buffer_load_dwordx4 v110, s[48:51], s65 offen offset:1024 lds\n"
    "ds_read_b64_tr_b16 a[222:223], v16 offset:12800\n"
    "v_mfma_f32_16x16x32_bf16 v[196:199], a[188:191], a[108:111], v[196:199]\n"
    "buffer_load_dwordx4 v110, s[48:51], s64 offen offset:2048 lds\n"
    "v_mfma_f32_16x16x32_bf16 v[200:203], a[188:191], a[112:115], v[200:203]\n"
    "v_mfma_f32_16x16x32_bf16 v[204:207], a[188:191], a[116:119], v[204:207]\n"
    "s_waitcnt vmcnt(14) lgkmcnt(0)\n"
    "s_barrier\n"
    "v_mfma_f32_16x16x32_bf16 v[128:131], a[192:195], a[120:123], v[128:131]\n"
    "s_add_u32 s66, s35, 6\n"
    "s_and_b32 s66, s66, 7\n"
    "s_lshl_b32 s66, s66, 19\n"
    "v_mfma_f32_16x16x32_bf16 v[132:135], a[192:195], a[124:127], v[132:135]\n"
    "s_add_u32 m0, s56, 8192\n"
    "s_add_u32 s64, s66, 0\n"
    "s_add_u32 s67, s68, 30720\n"
    "v_mfma_f32_16x16x32_bf16 v[136:139], a[192:195], a[128:131], v[136:139]\n"
    "buffer_load_dwordx4 v108, s[44:47], s64 offen offset:0 lds\n"
    "v_add_u32 v114, s67, v210\n"
    "v_mfma_f32_16x16x32_bf16 v[140:143], a[192:195], a[132:135], v[140:143]\n"
    "ds_read_b128 a[80:83], v114 offset:0\n"
    "v_xor_b32 v115, 64, v114\n"
    "v_mfma_f32_16x16x32_bf16 v[144:147], a[192:195], a[136:139], v[144:147]\n"
    "ds_read_b128 a[84:87], v114 offset:2048\n"
    "s_add_u32 s65, s66, 64512\n"
    "v_mfma_f32_16x16x32_bf16 v[148:151], a[196:199], a[120:123], v[148:151]\n"
    "buffer_load_dwordx4 v109, s[44:47], s65 offen offset:1024 lds\n"
    "s_add_u32 s64, s66, 129024\n"
    "v_mfma_f32_16x16x32_bf16 v[152:155], a[196:199], a[124:127], v[152:155]\n"
    "ds_read_b128 a[88:91], v114 offset:4096\n"
    "s_add_u32 s65, s66, 193536\n"
    "v_mfma_f32_16x16x32_bf16 v[156:159], a[196:199], a[128:131], v[156:159]\n"
    "buffer_load_dwordx4 v108, s[44:47], s64 offen offset:2048 lds\n"
    "s_add_u32 s64, s66, 262144\n"
    "v_mfma_f32_16x16x32_bf16 v[160:163], a[196:199], a[132:135], v[160:163]\n"
    "ds_read_b128 a[92:95], v114 offset:6144\n"
    "v_mfma_f32_16x16x32_bf16 v[164:167], a[196:199], a[136:139], v[164:167]\n"
    "ds_read_b128 a[96:99], v114 offset:8192\n"
    "v_mfma_f32_16x16x32_bf16 v[168:171], a[200:203], a[120:123], v[168:171]\n"
    "ds_read_b64_tr_b16 a[160:161], v13 offset:16384\n"
    "v_mfma_f32_16x16x32_bf16 v[172:175], a[200:203], a[124:127], v[172:175]\n"
    "buffer_load_dwordx4 v109, s[44:47], s65 offen offset:3072 lds\n"
    "s_add_u32 m0, s56, 12288\n"
    "v_mfma_f32_16x16x32_bf16 v[176:179], a[200:203], a[128:131], v[176:179]\n"
    "ds_read_b64_tr_b16 a[162:163], v13 offset:16896\n"
    "s_add_u32 s65, s66, 326656\n"
    "v_mfma_f32_16x16x32_bf16 v[180:183], a[200:203], a[132:135], v[180:183]\n"
    "ds_read_b64_tr_b16 a[164:165], v14 offset:16384\n"
    "v_mfma_f32_16x16x32_bf16 v[184:187], a[200:203], a[136:139], v[184:187]\n"
    "ds_read_b64_tr_b16 a[166:167], v14 offset:16896\n"
    "v_mfma_f32_16x16x32_bf16 v[188:191], a[204:207], a[120:123], v[188:191]\n"
    "buffer_load_dwordx4 v108, s[44:47], s64 offen offset:0 lds\n"
    "s_add_u32 s64, s66, 391168\n"
    "v_mfma_f32_16x16x32_bf16 v[192:195], a[204:207], a[124:127], v[192:195]\n"
    "ds_read_b64_tr_b16 a[168:169], v15 offset:16384\n"
    "v_mfma_f32_16x16x32_bf16 v[196:199], a[204:207], a[128:131], v[196:199]\n"
    "ds_read_b64_tr_b16 a[170:171], v15 offset:16896\n"
    "v_mfma_f32_16x16x32_bf16 v[200:203], a[204:207], a[132:135], v[200:203]\n"
    "ds_read_b64_tr_b16 a[172:173], v16 offset:16384\n"
    "v_mfma_f32_16x16x32_bf16 v[204:207], a[204:207], a[136:139], v[204:207]\n"
    "ds_read_b64_tr_b16 a[174:175], v16 offset:16896\n"
    "v_mfma_f32_16x16x32_bf16 v[128:131], a[208:211], a[140:143], v[128:131]\n"
    "buffer_load_dwordx4 v109, s[44:47], s65 offen offset:1024 lds\n"
    "s_add_u32 s65, s66, 455680\n"
    "v_mfma_f32_16x16x32_bf16 v[132:135], a[208:211], a[144:147], v[132:135]\n"
    "ds_read_b128 a[100:103], v115 offset:0\n"
    "s_add_u32 s66, s35, 7\n"
    "v_mfma_f32_16x16x32_bf16 v[136:139], a[208:211], a[148:151], v[136:139]\n"
    "ds_read_b128 a[104:107], v115 offset:2048\n"
    "s_and_b32 s66, s66, 7\n"
    "v_mfma_f32_16x16x32_bf16 v[140:143], a[208:211], a[152:155], v[140:143]\n"
    "buffer_load_dwordx4 v108, s[44:47], s64 offen offset:2048 lds\n"
    "s_lshl_b32 s66, s66, 7\n"
    "v_mfma_f32_16x16x32_bf16 v[144:147], a[208:211], a[156:159], v[144:147]\n"
    "ds_read_b128 a[108:111], v115 offset:4096\n"
    "s_add_u32 s66, s66, s59\n"
    "v_mfma_f32_16x16x32_bf16 v[148:151], a[212:215], a[140:143], v[148:151]\n"
    "ds_read_b128 a[112:115], v115 offset:6144\n"
    "s_add_u32 s64, s66, 0\n"
    "v_mfma_f32_16x16x32_bf16 v[152:155], a[212:215], a[144:147], v[152:155]\n"
    "ds_read_b128 a[116:119], v115 offset:8192\n"
    "v_mfma_f32_16x16x32_bf16 v[156:159], a[212:215], a[148:151], v[156:159]\n"
    "ds_read_b64_tr_b16 a[176:177], v13 offset:20480\n"
    "v_mfma_f32_16x16x32_bf16 v[160:163], a[212:215], a[152:155], v[160:163]\n"
    "buffer_load_dwordx4 v109, s[44:47], s65 offen offset:3072 lds\n"
    "s_add_u32 m0, s58, 153600\n"
    "v_mfma_f32_16x16x32_bf16 v[164:167], a[212:215], a[156:159], v[164:167]\n"
    "ds_read_b64_tr_b16 a[178:179], v13 offset:20992\n"
    "s_add_u32 s65, s66, 7168\n"
    "v_mfma_f32_16x16x32_bf16 v[168:171], a[216:219], a[140:143], v[168:171]\n"
    "ds_read_b64_tr_b16 a[180:181], v14 offset:20480\n"
    "v_mfma_f32_16x16x32_bf16 v[172:175], a[216:219], a[144:147], v[172:175]\n"
    "ds_read_b64_tr_b16 a[182:183], v14 offset:20992\n"
    "v_mfma_f32_16x16x32_bf16 v[176:179], a[216:219], a[148:151], v[176:179]\n"
    "buffer_load_dwordx4 v110, s[48:51], s64 offen offset:0 lds\n"
    "s_add_u32 s64, s66, 14336\n"
    "v_mfma_f32_16x16x32_bf16 v[180:183], a[216:219], a[152:155], v[180:183]\n"
    "ds_read_b64_tr_b16 a[184:185], v15 offset:20480\n"
    "v_mfma_f32_16x16x32_bf16 v[184:187], a[216:219], a[156:159], v[184:187]\n"
    "ds_read_b64_tr_b16 a[186:187], v15 offset:20992\n"
    "v_mfma_f32_16x16x32_bf16 v[188:191], a[220:223], a[140:143], v[188:191]\n"
    "ds_read_b64_tr_b16 a[188:189], v16 offset:20480\n"
    "v_mfma_f32_16x16x32_bf16 v[192:195], a[220:223], a[144:147], v[192:195]\n"
    "buffer_load_dwordx4 v110, s[48:51], s65 offen offset:1024 lds\n"
    "ds_read_b64_tr_b16 a[190:191], v16 offset:20992\n"
    "v_mfma_f32_16x16x32_bf16 v[196:199], a[220:223], a[148:151], v[196:199]\n"
    "buffer_load_dwordx4 v110, s[48:51], s64 offen offset:2048 lds\n"
    "v_mfma_f32_16x16x32_bf16 v[200:203], a[220:223], a[152:155], v[200:203]\n"
    "v_mfma_f32_16x16x32_bf16 v[204:207], a[220:223], a[156:159], v[204:207]\n"
    "s_waitcnt vmcnt(14) lgkmcnt(0)\n"
    "s_barrier\n"
    "v_mfma_f32_16x16x32_bf16 v[128:131], a[160:163], a[80:83], v[128:131]\n"
    "s_add_u32 s66, s35, 7\n"
    "s_and_b32 s66, s66, 7\n"
    "s_lshl_b32 s66, s66, 19\n"
    "v_mfma_f32_16x16x32_bf16 v[132:135], a[160:163], a[84:87], v[132:135]\n"
    "s_add_u32 m0, s56, 16384\n"
    "s_add_u32 s64, s66, 0\n"
    "s_add_u32 s67, s68, 71680\n"
    "v_mfma_f32_16x16x32_bf16 v[136:139], a[160:163], a[88:91], v[136:139]\n"
    "buffer_load_dwordx4 v108, s[44:47], s64 offen offset:0 lds\n"
    "v_add_u32 v114, s67, v210\n"
    "v_mfma_f32_16x16x32_bf16 v[140:143], a[160:163], a[92:95], v[140:143]\n"
    "ds_read_b128 a[120:123], v114 offset:0\n"
    "v_xor_b32 v115, 64, v114\n"
    "v_mfma_f32_16x16x32_bf16 v[144:147], a[160:163], a[96:99], v[144:147]\n"
    "ds_read_b128 a[124:127], v114 offset:2048\n"
    "s_add_u32 s65, s66, 64512\n"
    "v_mfma_f32_16x16x32_bf16 v[148:151], a[164:167], a[80:83], v[148:151]\n"
    "buffer_load_dwordx4 v109, s[44:47], s65 offen offset:1024 lds\n"
    "s_add_u32 s64, s66, 129024\n"
    "v_mfma_f32_16x16x32_bf16 v[152:155], a[164:167], a[84:87], v[152:155]\n"
    "ds_read_b128 a[128:131], v114 offset:4096\n"
    "s_add_u32 s65, s66, 193536\n"
    "v_mfma_f32_16x16x32_bf16 v[156:159], a[164:167], a[88:91], v[156:159]\n"
    "ds_read_b128 a[132:135], v114 offset:6144\n"
    "v_mfma_f32_16x16x32_bf16 v[160:163], a[164:167], a[92:95], v[160:163]\n"
    "ds_read_b128 a[136:139], v114 offset:8192\n"
    "v_mfma_f32_16x16x32_bf16 v[164:167], a[164:167], a[96:99], v[164:167]\n"
    "ds_read_b64_tr_b16 a[192:193], v13 offset:0\n"
    "v_mfma_f32_16x16x32_bf16 v[168:171], a[168:171], a[80:83], v[168:171]\n"
    "buffer_load_dwordx4 v108, s[44:47], s64 offen offset:2048 lds\n"
    "s_add_u32 s64, s66, 262144\n"
    "v_mfma_f32_16x16x32_bf16 v[172:175], a[168:171], a[84:87], v[172:175]\n"
    "ds_read_b64_tr_b16 a[194:195], v13 offset:512\n"
    "v_mfma_f32_16x16x32_bf16 v[176:179], a[168:171], a[88:91], v[176:179]\n"
    "ds_read_b64_tr_b16 a[196:197], v14 offset:0\n"
    "v_mfma_f32_16x16x32_bf16 v[180:183], a[168:171], a[92:95], v[180:183]\n"
    "ds_read_b64_tr_b16 a[198:199], v14 offset:512\n"
    "v_mfma_f32_16x16x32_bf16 v[184:187], a[168:171], a[96:99], v[184:187]\n"
    "ds_read_b64_tr_b16 a[200:201], v15 offset:0\n"
    "v_mfma_f32_16x16x32_bf16 v[188:191], a[172:175], a[80:83], v[188:191]\n"
    "buffer_load_dwordx4 v109, s[44:47], s65 offen offset:3072 lds\n"
    "s_add_u32 m0, s56, 20480\n"
    "v_mfma_f32_16x16x32_bf16 v[192:195], a[172:175], a[84:87], v[192:195]\n"
    "ds_read_b64_tr_b16 a[202:203], v15 offset:512\n"
    "s_add_u32 s65, s66, 326656\n"
    "v_mfma_f32_16x16x32_bf16 v[196:199], a[172:175], a[88:91], v[196:199]\n"
    "ds_read_b64_tr_b16 a[204:205], v16 offset:0\n"
    "v_mfma_f32_16x16x32_bf16 v[200:203], a[172:175], a[92:95], v[200:203]\n"
    "ds_read_b64_tr_b16 a[206:207], v16 offset:512\n"
    "v_mfma_f32_16x16x32_bf16 v[204:207], a[172:175], a[96:99], v[204:207]\n"
    "ds_read_b128 a[140:143], v115 offset:0\n"
    "v_mfma_f32_16x16x32_bf16 v[128:131], a[176:179], a[100:103], v[128:131]\n"
    "ds_read_b128 a[144:147], v115 offset:2048\n"
    "v_mfma_f32_16x16x32_bf16 v[132:135], a[176:179], a[104:107], v[132:135]\n"
    "buffer_load_dwordx4 v108, s[44:47], s64 offen offset:0 lds\n"
    "s_add_u32 s64, s66, 391168\n"
    "v_mfma_f32_16x16x32_bf16 v[136:139], a[176:179], a[108:111], v[136:139]\n"
    "ds_read_b128 a[148:151], v115 offset:4096\n"
    "v_mfma_f32_16x16x32_bf16 v[140:143], a[176:179], a[112:115], v[140:143]\n"
    "ds_read_b128 a[152:155], v115 offset:6144\n"
    "v_mfma_f32_16x16x32_bf16 v[144:147], a[176:179], a[116:119], v[144:147]\n"
    "ds_read_b128 a[156:159], v115 offset:8192\n"
    "v_mfma_f32_16x16x32_bf16 v[148:151], a[180:183], a[100:103], v[148:151]\n"
    "ds_read_b64_tr_b16 a[208:209], v13 offset:4096\n"
    "v_mfma_f32_16x16x32_bf16 v[152:155], a[180:183], a[104:107], v[152:155]\n"
    "buffer_load_dwordx4 v109, s[44:47], s65 offen offset:1024 lds\n"
    "s_add_u32 s65, s66, 455680\n"
    "v_mfma_f32_16x16x32_bf16 v[156:159], a[180:183], a[108:111], v[156:159]\n"
    "ds_read_b64_tr_b16 a[210:211], v13 offset:4608\n"
    "v_mfma_f32_16x16x32_bf16 v[160:163], a[180:183], a[112:115], v[160:163]\n"
    "ds_read_b64_tr_b16 a[212:213], v14 offset:4096\n"
    "v_mfma_f32_16x16x32_bf16 v[164:167], a[180:183], a[116:119], v[164:167]\n"
    "ds_read_b64_tr_b16 a[214:215], v14 offset:4608\n"
    "v_mfma_f32_16x16x32_bf16 v[168:171], a[184:187], a[100:103], v[168:171]\n"
    "ds_read_b64_tr_b16 a[216:217], v15 offset:4096\n"
    "v_mfma_f32_16x16x32_bf16 v[172:175], a[184:187], a[104:107], v[172:175]\n"
    "buffer_load_dwordx4 v108, s[44:47], s64 offen offset:2048 lds\n"
    "v_mfma_f32_16x16x32_bf16 v[176:179], a[184:187], a[108:111], v[176:179]\n"
    "ds_read_b64_tr_b16 a[218:219], v15 offset:4608\n"
    "v_mfma_f32_16x16x32_bf16 v[180:183], a[184:187], a[112:115], v[180:183]\n"
    "ds_read_b64_tr_b16 a[220:221], v16 offset:4096\n"
    "v_mfma_f32_16x16x32_bf16 v[184:187], a[184:187], a[116:119], v[184:187]\n"
    "ds_read_b64_tr_b16 a[222:223], v16 offset:4608\n"
    "v_mfma_f32_16x16x32_bf16 v[188:191], a[188:191], a[100:103], v[188:191]\n"
    "buffer_load_dwordx4 v109, s[44:47], s65 offen offset:3072 lds\n"
    "v_mfma_f32_16x16x32_bf16 v[192:195], a[188:191], a[104:107], v[192:195]\n"
    "v_mfma_f32_16x16x32_bf16 v[196:199], a[188:191], a[108:111], v[196:199]\n"
    "v_mfma_f32_16x16x32_bf16 v[200:203], a[188:191], a[112:115], v[200:203]\n"
    "v_mfma_f32_16x16x32_bf16 v[204:207], a[188:191], a[116:119], v[204:207]\n"
    "s_waitcnt vmcnt(11) lgkmcnt(0)\n"
    "s_barrier\n"
    "s_xor_b32 s28, s26, 0x7f4a7c15\n"
    "s_xor_b32 s29, s27, 0x9e3779b9\n"
    "v_mov_b32 v44, s28\n"
    "v_mov_b32 v45, s29\n"
    "buffer_store_dwordx2 v[44:45], v46, s[52:55], 0 offen sc1\n"
    "s_mov_b32 s48, s36\n"
    "s_mov_b32 s49, s37\n"
    "s_mov_b32 s50, s38\n"
    "s_mov_b32 s51, s39\n"
    "v_mov_b32 v9, v3\n"
    "v_mov_b32 v10, v4\n"
    "v_mov_b32 v11, v5\n"
    "v_mov_b32 v12, v6\n"
    "s_cmp_eq_u32 s34, 0\n"
    "s_cselect_b32 s70, 0, 0\n"
    "s_add_u32 s60, s69, s70\n"
    "s_and_b32 s60, s60, 15\n"
    "s_lshl_b32 s60, s60, 13\n"
    "s_cmp_lt_u32 s70, 16\n"
    "s_cselect_b32 s60, s60, 0x7ff00000\n"
    "s_add_u32 s61, s60, 4096\n"
    "v_mfma_f32_16x16x32_bf16 v[128:131], a[192:195], a[120:123], v[128:131]\n"
    "s_add_u32 m0, s56, 0\n"
    "s_add_u32 s67, s68, 112640\n"
    "v_mfma_f32_16x16x32_bf16 v[132:135], a[192:195], a[124:127], v[132:135]\n"
    "buffer_load_dwordx4 v9, s[48:51], s60 offen offset:0 nt lds\n"
    "v_add_u32 v114, s67, v210\n"
    "v_mfma_f32_16x16x32_bf16 v[136:139], a[192:195], a[128:131], v[136:139]\n"
    "v_xor_b32 v115, 64, v114\n"
    "v_mfma_f32_16x16x32_bf16 v[140:143], a[192:195], a[132:135], v[140:143]\n"
    "ds_read_b128 a[80:83], v114 offset:0\n"
    "v_mfma_f32_16x16x32_bf16 v[144:147], a[192:195], a[136:139], v[144:147]\n"
    "ds_read_b128 a[84:87], v114 offset:2048\n"
    "v_mfma_f32_16x16x32_bf16 v[148:151], a[196:199], a[120:123], v[148:151]\n"
    "buffer_load_dwordx4 v9, s[48:51], s60 offen offset:1024 nt lds\n"
    "v_mfma_f32_16x16x32_bf16 v[152:155], a[196:199], a[124:127], v[152:155]\n"
    "ds_read_b128 a[88:91], v114 offset:4096\n"
    "v_mfma_f32_16x16x32_bf16 v[156:159], a[196:199], a[128:131], v[156:159]\n"
    "ds_read_b128 a[92:95], v114 offset:6144\n"
    "v_mfma_f32_16x16x32_bf16 v[160:163], a[196:199], a[132:135], v[160:163]\n"
    "ds_read_b128 a[96:99], v114 offset:8192\n"
    "v_mfma_f32_16x16x32_bf16 v[164:167], a[196:199], a[136:139], v[164:167]\n"
    "ds_read_b64_tr_b16 a[160:161], v13 offset:8192\n"
    "v_mfma_f32_16x16x32_bf16 v[168:171], a[200:203], a[120:123], v[168:171]\n"
    "buffer_load_dwordx4 v10, s[48:51], s60 offen offset:2048 nt lds\n"
    "v_mfma_f32_16x16x32_bf16 v[172:175], a[200:203], a[124:127], v[172:175]\n"
    "ds_read_b64_tr_b16 a[162:163], v13 offset:8704\n"
    "v_mfma_f32_16x16x32_bf16 v[176:179], a[200:203], a[128:131], v[176:179]\n"
    "ds_read_b64_tr_b16 a[164:165], v14 offset:8192\n"
    "v_mfma_f32_16x16x32_bf16 v[180:183], a[200:203], a[132:135], v[180:183]\n"
    "ds_read_b64_tr_b16 a[166:167], v14 offset:8704\n"
    "v_mfma_f32_16x16x32_bf16 v[184:187], a[200:203], a[136:139], v[184:187]\n"
    "ds_read_b64_tr_b16 a[168:169], v15 offset:8192\n"
    "v_mfma_f32_16x16x32_bf16 v[188:191], a[204:207], a[120:123], v[188:191]\n"
    "buffer_load_dwordx4 v10, s[48:51], s60 offen offset:3072 nt lds\n"
    "s_add_u32 m0, s56, 4096\n"
    "v_mfma_f32_16x16x32_bf16 v[192:195], a[204:207], a[124:127], v[192:195]\n"
    "ds_read_b64_tr_b16 a[170:171], v15 offset:8704\n"
    "v_mfma_f32_16x16x32_bf16 v[196:199], a[204:207], a[128:131], v[196:199]\n"
    "ds_read_b64_tr_b16 a[172:173], v16 offset:8192\n"
    "v_mfma_f32_16x16x32_bf16 v[200:203], a[204:207], a[132:135], v[200:203]\n"
    "ds_read_b64_tr_b16 a[174:175], v16 offset:8704\n"
    "v_mfma_f32_16x16x32_bf16 v[204:207], a[204:207], a[136:139], v[204:207]\n"
    "ds_read_b128 a[100:103], v115 offset:0\n"
    "v_mfma_f32_16x16x32_bf16 v[128:131], a[208:211], a[140:143], v[128:131]\n"
    "ds_read_b128 a[104:107], v115 offset:2048\n"
    "v_mfma_f32_16x16x32_bf16 v[132:135], a[208:211], a[144:147], v[132:135]\n"
    "buffer_load_dwordx4 v11, s[48:51], s61 offen offset:0 nt lds\n"
    "v_mfma_f32_16x16x32_bf16 v[136:139], a[208:211], a[148:151], v[136:139]\n"
    "ds_read_b128 a[108:111], v115 offset:4096\n"
    "v_mfma_f32_16x16x32_bf16 v[140:143], a[208:211], a[152:155], v[140:143]\n"
    "ds_read_b128 a[112:115], v115 offset:6144\n"
    "v_mfma_f32_16x16x32_bf16 v[144:147], a[208:211], a[156:159], v[144:147]\n"
    "ds_read_b128 a[116:119], v115 offset:8192\n"
    "v_mfma_f32_16x16x32_bf16 v[148:151], a[212:215], a[140:143], v[148:151]\n"
    "ds_read_b64_tr_b16 a[176:177], v13 offset:12288\n"
    "v_mfma_f32_16x16x32_bf16 v[152:155], a[212:215], a[144:147], v[152:155]\n"
    "buffer_load_dwordx4 v11, s[48:51], s61 offen offset:1024 nt lds\n"
    "v_mfma_f32_16x16x32_bf16 v[156:159], a[212:215], a[148:151], v[156:159]\n"
    "ds_read_b64_tr_b16 a[178:179], v13 offset:12800\n"
    "v_mfma_f32_16x16x32_bf16 v[160:163], a[212:215], a[152:155], v[160:163]\n"
    "ds_read_b64_tr_b16 a[180:181], v14 offset:12288\n"
    "v_mfma_f32_16x16x32_bf16 v[164:167], a[212:215], a[156:159], v[164:167]\n"
    "ds_read_b64_tr_b16 a[182:183], v14 offset:12800\n"
    "v_mfma_f32_16x16x32_bf16 v[168:171], a[216:219], a[140:143], v[168:171]\n"
    "ds_read_b64_tr_b16 a[184:185], v15 offset:12288\n"
    "v_mfma_f32_16x16x32_bf16 v[172:175], a[216:219], a[144:147], v[172:175]\n"
    "buffer_load_dwordx4 v12, s[48:51], s61 offen offset:2048 nt lds\n"
    "v_mfma_f32_16x16x32_bf16 v[176:179], a[216:219], a[148:151], v[176:179]\n"
    "ds_read_b64_tr_b16 a[186:187], v15 offset:12800\n"
    "v_mfma_f32_16x16x32_bf16 v[180:183], a[216:219], a[152:155], v[180:183]\n"
    "ds_read_b64_tr_b16 a[188:189], v16 offset:12288\n"
    "v_mfma_f32_16x16x32_bf16 v[184:187], a[216:219], a[156:159], v[184:187]\n"
    "ds_read_b64_tr_b16 a[190:191], v16 offset:12800\n"
    "v_mfma_f32_16x16x32_bf16 v[188:191], a[220:223], a[140:143], v[188:191]\n"
    "buffer_load_dwordx4 v12, s[48:51], s61 offen offset:3072 nt lds\n"
    "v_mfma_f32_16x16x32_bf16 v[192:195], a[220:223], a[144:147], v[192:195]\n"
    "v_mfma_f32_16x16x32_bf16 v[196:199], a[220:223], a[148:151], v[196:199]\n"
    "v_mfma_f32_16x16x32_bf16 v[200:203], a[220:223], a[152:155], v[200:203]\n"
    "v_mfma_f32_16x16x32_bf16 v[204:207], a[220:223], a[156:159], v[204:207]\n"
    "s_waitcnt vmcnt(9) lgkmcnt(0)\n"
    "s_barrier\n"
    "s_cmp_eq_u32 s34, 0\n"
    "s_cselect_b32 s48, s36, s40\n"
    "s_cselect_b32 s49, s37, s41\n"
    "s_cselect_b32 s50, s38, s42\n"
    "s_cselect_b32 s51, s39, s43\n"
    "s_cselect_b64 vcc, -1, 0\n"
    "v_cndmask_b32 v9, v7, v3, vcc\n"
    "v_cndmask_b32 v10, v8, v4, vcc\n"
    "v_cndmask_b32 v11, v7, v5, vcc\n"
    "v_cndmask_b32 v12, v8, v6, vcc\n"
    "s_cmp_eq_u32 s34, 0\n"
    "s_cselect_b32 s70, 1, 0\n"
    "s_add_u32 s60, s69, s70\n"
    "s_and_b32 s60, s60, 15\n"
    "s_lshl_b32 s60, s60, 13\n"
    "s_cmp_lt_u32 s70, 16\n"
    "s_cselect_b32 s60, s60, 0x7ff00000\n"
    "s_add_u32 s61, s60, 4096\n"
    "v_mfma_f32_16x16x32_bf16 v[128:131], a[160:163], a[80:83], v[128:131]\n"
    "s_add_u32 m0, s56, 8192\n"
    "s_add_u32 s67, s68, 153600\n"
    "v_mfma_f32_16x16x32_bf16 v[132:135], a[160:163], a[84:87], v[132:135]\n"
    "buffer_load_dwordx4 v9, s[48:51], s60 offen offset:0 nt lds\n"
    "v_add_u32 v114, s67, v210\n"
    "v_mfma_f32_16x16x32_bf16 v[136:139], a[160:163], a[88:91], v[136:139]\n"
    "v_xor_b32 v115, 64, v114\n"
    "v_mfma_f32_16x16x32_bf16 v[140:143], a[160:163], a[92:95], v[140:143]\n"
    "ds_read_b128 a[120:123], v114 offset:0\n"
    "v_mfma_f32_16x16x32_bf16 v[144:147], a[160:163], a[96:99], v[144:147]\n"
    "ds_read_b128 a[124:127], v114 offset:2048\n"
    "v_mfma_f32_16x16x32_bf16 v[148:151], a[164:167], a[80:83], v[148:151]\n"
    "buffer_load_dwordx4 v9, s[48:51], s60 offen offset:1024 nt lds\n"
    "v_mfma_f32_16x16x32_bf16 v[152:155], a[164:167], a[84:87], v[152:155]\n"
    "ds_read_b128 a[128:131], v114 offset:4096\n"
    "v_mfma_f32_16x16x32_bf16 v[156:159], a[164:167], a[88:91], v[156:159]\n"
    "ds_read_b128 a[132:135], v114 offset:6144\n"
    "v_mfma_f32_16x16x32_bf16 v[160:163], a[164:167], a[92:95], v[160:163]\n"
    "ds_read_b128 a[136:139], v114 offset:8192\n"
    "v_mfma_f32_16x16x32_bf16 v[164:167], a[164:167], a[96:99], v[164:167]\n"
    "ds_read_b64_tr_b16 a[192:193], v13 offset:16384\n"
    "v_mfma_f32_16x16x32_bf16 v[168:171], a[168:171], a[80:83], v[168:171]\n"
    "buffer_load_dwordx4 v10, s[48:51], s60 offen offset:2048 nt lds\n"
    "v_mfma_f32_16x16x32_bf16 v[172:175], a[168:171], a[84:87], v[172:175]\n"
    "ds_read_b64_tr_b16 a[194:195], v13 offset:16896\n"
    "v_mfma_f32_16x16x32_bf16 v[176:179], a[168:171], a[88:91], v[176:179]\n"
    "ds_read_b64_tr_b16 a[196:197], v14 offset:16384\n"
    "v_mfma_f32_16x16x32_bf16 v[180:183], a[168:171], a[92:95], v[180:183]\n"
    "ds_read_b64_tr_b16 a[198:199], v14 offset:16896\n"
    "v_mfma_f32_16x16x32_bf16 v[184:187], a[168:171], a[96:99], v[184:187]\n"
    "ds_read_b64_tr_b16 a[200:201], v15 offset:16384\n"
    "v_mfma_f32_16x16x32_bf16 v[188:191], a[172:175], a[80:83], v[188:191]\n"
    "buffer_load_dwordx4 v10, s[48:51], s60 offen offset:3072 nt lds\n"
    "s_add_u32 m0, s56, 12288\n"
    "v_mfma_f32_16x16x32_bf16 v[192:195], a[172:175], a[84:87], v[192:195]\n"
    "ds_read_b64_tr_b16 a[202:203], v15 offset:16896\n"
    "v_mfma_f32_16x16x32_bf16 v[196:199], a[172:175], a[88:91], v[196:199]\n"
    "ds_read_b64_tr_b16 a[204:205], v16 offset:16384\n"
    "v_mfma_f32_16x16x32_bf16 v[200:203], a[172:175], a[92:95], v[200:203]\n"
    "ds_read_b64_tr_b16 a[206:207], v16 offset:16896\n"
    "v_mfma_f32_16x16x32_bf16 v[204:207], a[172:175], a[96:99], v[204:207]\n"
    "ds_read_b128 a[140:143], v115 offset:0\n"
    "v_mfma_f32_16x16x32_bf16 v[128:131], a[176:179], a[100:103], v[128:131]\n"
    "ds_read_b128 a[144:147], v115 offset:2048\n"
    "v_mfma_f32_16x16x32_bf16 v[132:135], a[176:179], a[104:107], v[132:135]\n"
    "buffer_load_dwordx4 v11, s[48:51], s61 offen offset:0 nt lds\n"
    "v_mfma_f32_16x16x32_bf16 v[136:139], a[176:179], a[108:111], v[136:139]\n"
    "ds_read_b128 a[148:151], v115 offset:4096\n"
    "v_mfma_f32_16x16x32_bf16 v[140:143], a[176:179], a[112:115], v[140:143]\n"
    "ds_read_b128 a[152:155], v115 offset:6144\n"
    "v_mfma_f32_16x16x32_bf16 v[144:147], a[176:179], a[116:119], v[144:147]\n"
    "ds_read_b128 a[156:159], v115 offset:8192\n"
    "v_mfma_f32_16x16x32_bf16 v[148:151], a[180:183], a[100:103], v[148:151]\n"
    "ds_read_b64_tr_b16 a[208:209], v13 offset:20480\n"
    "v_mfma_f32_16x16x32_bf16 v[152:155], a[180:183], a[104:107], v[152:155]\n"
    "buffer_load_dwordx4 v11, s[48:51], s61 offen offset:1024 nt lds\n"
    "v_mfma_f32_16x16x32_bf16 v[156:159], a[180:183], a[108:111], v[156:159]\n"
    "ds_read_b64_tr_b16 a[210:211], v13 offset:20992\n"
    "v_mfma_f32_16x16x32_bf16 v[160:163], a[180:183], a[112:115], v[160:163]\n"
    "ds_read_b64_tr_b16 a[212:213], v14 offset:20480\n"
    "v_mfma_f32_16x16x32_bf16 v[164:167], a[180:183], a[116:119], v[164:167]\n"
    "ds_read_b64_tr_b16 a[214:215], v14 offset:20992\n"
    "v_mfma_f32_16x16x32_bf16 v[168:171], a[184:187], a[100:103], v[168:171]\n"
    "ds_read_b64_tr_b16 a[216:217], v15 offset:20480\n"
    "v_mfma_f32_16x16x32_bf16 v[172:175], a[184:187], a[104:107], v[172:175]\n"
    "buffer_load_dwordx4 v12, s[48:51], s61 offen offset:2048 nt lds\n"
    "v_mfma_f32_16x16x32_bf16 v[176:179], a[184:187], a[108:111], v[176:179]\n"
    "ds_read_b64_tr_b16 a[218:219], v15 offset:20992\n"
    "v_mfma_f32_16x16x32_bf16 v[180:183], a[184:187], a[112:115], v[180:183]\n"
    "ds_read_b64_tr_b16 a[220:221], v16 offset:20480\n"
    "v_mfma_f32_16x16x32_bf16 v[184:187], a[184:187], a[116:119], v[184:187]\n"
    "ds_read_b64_tr_b16 a[222:223], v16 offset:20992\n"
    "v_mfma_f32_16x16x32_bf16 v[188:191], a[188:191], a[100:103], v[188:191]\n"
    "buffer_load_dwordx4 v12, s[48:51], s61 offen offset:3072 nt lds\n"
    "v_mfma_f32_16x16x32_bf16 v[192:195], a[188:191], a[104:107], v[192:195]\n"
    "v_mfma_f32_16x16x32_bf16 v[196:199], a[188:191], a[108:111], v[196:199]\n"
    "v_mfma_f32_16x16x32_bf16 v[200:203], a[188:191], a[112:115], v[200:203]\n"
    "v_mfma_f32_16x16x32_bf16 v[204:207], a[188:191], a[116:119], v[204:207]\n"
    "s_waitcnt lgkmcnt(0)\n"
    "s_barrier\n"
    "s_mov_b32 s48, s36\n"
    "s_mov_b32 s49, s37\n"
    "s_mov_b32 s50, s38\n"
    "s_mov_b32 s51, s39\n"
    "v_mov_b32 v9, v3\n"
    "v_mov_b32 v10, v4\n"
    "v_mov_b32 v11, v5\n"
    "v_mov_b32 v12, v6\n"
    "s_cmp_eq_u32 s34, 0\n"
    "s_cselect_b32 s70, 2, 1\n"
    "s_add_u32 s60, s69, s70\n"
    "s_and_b32 s60, s60, 15\n"
    "s_lshl_b32 s60, s60, 13\n"
    "s_cmp_lt_u32 s70, 16\n"
    "s_cselect_b32 s60, s60, 0x7ff00000\n"
    "s_add_u32 s61, s60, 4096\n"
    "v_mfma_f32_16x16x32_bf16 v[128:131], a[192:195], a[120:123], v[128:131]\n"
    "s_add_u32 m0, s56, 16384\n"
    "v_mfma_f32_16x16x32_bf16 v[132:135], a[192:195], a[124:127], v[132:135]\n"
    "buffer_load_dwordx4 v9, s[48:51], s60 offen offset:0 nt lds\n"
    "v_mfma_f32_16x16x32_bf16 v[136:139], a[192:195], a[128:131], v[136:139]\n"
    "v_mfma_f32_16x16x32_bf16 v[140:143], a[192:195], a[132:135], v[140:143]\n"
    "v_mfma_f32_16x16x32_bf16 v[144:147], a[192:195], a[136:139], v[144:147]\n"
    "v_mfma_f32_16x16x32_bf16 v[148:151], a[196:199], a[120:123], v[148:151]\n"
    "buffer_load_dwordx4 v9, s[48:51], s60 offen offset:1024 nt lds\n"
    "v_mfma_f32_16x16x32_bf16 v[152:155], a[196:199], a[124:127], v[152:155]\n"
    "v_mfma_f32_16x16x32_bf16 v[156:159], a[196:199], a[128:131], v[156:159]\n"
    "v_mfma_f32_16x16x32_bf16 v[160:163], a[196:199], a[132:135], v[160:163]\n"
    "v_mfma_f32_16x16x32_bf16 v[164:167], a[196:199], a[136:139], v[164:167]\n"
    "v_mfma_f32_16x16x32_bf16 v[168:171], a[200:203], a[120:123], v[168:171]\n"
    "buffer_load_dwordx4 v10, s[48:51], s60 offen offset:2048 nt lds\n"
    "v_mfma_f32_16x16x32_bf16 v[172:175], a[200:203], a[124:127], v[172:175]\n"
    "v_mfma_f32_16x16x32_bf16 v[176:179], a[200:203], a[128:131], v[176:179]\n"
    "v_mfma_f32_16x16x32_bf16 v[180:183], a[200:203], a[132:135], v[180:183]\n"
    "v_mfma_f32_16x16x32_bf16 v[184:187], a[200:203], a[136:139], v[184:187]\n"
    "v_mfma_f32_16x16x32_bf16 v[188:191], a[204:207], a[120:123], v[188:191]\n"
    "buffer_load_dwordx4 v10, s[48:51], s60 offen offset:3072 nt lds\n"
    "s_add_u32 m0, s56, 20480\n"
    "v_mfma_f32_16x16x32_bf16 v[192:195], a[204:207], a[124:127], v[192:195]\n"
    "v_mfma_f32_16x16x32_bf16 v[196:199], a[204:207], a[128:131], v[196:199]\n"
    "v_mfma_f32_16x16x32_bf16 v[200:203], a[204:207], a[132:135], v[200:203]\n"
    "v_mfma_f32_16x16x32_bf16 v[204:207], a[204:207], a[136:139], v[204:207]\n"
    "v_mfma_f32_16x16x32_bf16 v[128:131], a[208:211], a[140:143], v[128:131]\n"
    "buffer_load_dwordx4 v11, s[48:51], s61 offen offset:0 nt lds\n"
    "v_mfma_f32_16x16x32_bf16 v[132:135], a[208:211], a[144:147], v[132:135]\n"
    "v_mfma_f32_16x16x32_bf16 v[136:139], a[208:211], a[148:151], v[136:139]\n"
    "v_mfma_f32_16x16x32_bf16 v[140:143], a[208:211], a[152:155], v[140:143]\n"
    "v_mfma_f32_16x16x32_bf16 v[144:147], a[208:211], a[156:159], v[144:147]\n"
    "v_mfma_f32_16x16x32_bf16 v[148:151], a[212:215], a[140:143], v[148:151]\n"
    "buffer_load_dwordx4 v11, s[48:51], s61 offen offset:1024 nt lds\n"
    "v_mfma_f32_16x16x32_bf16 v[152:155], a[212:215], a[144:147], v[152:155]\n"
    "v_mfma_f32_16x16x32_bf16 v[156:159], a[212:215], a[148:151], v[156:159]\n"
    "v_mfma_f32_16x16x32_bf16 v[160:163], a[212:215], a[152:155], v[160:163]\n"
    "v_mfma_f32_16x16x32_bf16 v[164:167], a[212:215], a[156:159], v[164:167]\n"
    "v_mfma_f32_16x16x32_bf16 v[168:171], a[216:219], a[140:143], v[168:171]\n"
    "buffer_load_dwordx4 v12, s[48:51], s61 offen offset:2048 nt lds\n"
    "v_mfma_f32_16x16x32_bf16 v[172:175], a[216:219], a[144:147], v[172:175]\n"
    "v_mfma_f32_16x16x32_bf16 v[176:179], a[216:219], a[148:151], v[176:179]\n"
    "v_mfma_f32_16x16x32_bf16 v[180:183], a[216:219], a[152:155], v[180:183]\n"
    "v_mfma_f32_16x16x32_bf16 v[184:187], a[216:219], a[156:159], v[184:187]\n"
    "v_mfma_f32_16x16x32_bf16 v[188:191], a[220:223], a[140:143], v[188:191]\n"
    "buffer_load_dwordx4 v12, s[48:51], s61 offen offset:3072 nt lds\n"
    "v_mfma_f32_16x16x32_bf16 v[192:195], a[220:223], a[144:147], v[192:195]\n"
    "v_mfma_f32_16x16x32_bf16 v[196:199], a[220:223], a[148:151], v[196:199]\n"
    "v_mfma_f32_16x16x32_bf16 v[200:203], a[220:223], a[152:155], v[200:203]\n"
    "v_mfma_f32_16x16x32_bf16 v[204:207], a[220:223], a[156:159], v[204:207]\n"
    "s_mov_b32 s62, 0\n"
    "s_mov_b32 s74, 0xff800000\n"
    "s_cmp_eq_u32 s34, 0\n"
    "s_cbranch_scc1 LPREA%=\n"
    "s_mov_b32 s58, s56\n"
    "s_add_u32 s59, s56, 8192\n"
    "v_add_u32 v9, s58, v1\n"
    "v_xor_b32 v10, 0x40, v9\n"
    "v_xor_b32 v11, 0x80, v9\n"
    "v_xor_b32 v12, 0xc0, v9\n"
    "v_add_u32 v13, s59, v2\n"
    "v_xor_b32 v14, 0x20, v13\n"
    "v_xor_b32 v15, 0x40, v13\n"
    "v_xor_b32 v16, 0x60, v13\n"
    "v_xor_b32 v17, 0x80, v13\n"
    "v_xor_b32 v18, 0xa0, v13\n"
    "v_xor_b32 v19, 0xc0, v13\n"
    "v_xor_b32 v20, 0xe0, v13\n"
    "s_waitcnt vmcnt(16)\n"
    "ds_read_b128 a[160:163], v9 offset:0\n"
    "ds_read_b128 a[176:179], v9 offset:1024\n"
    "ds_read_b128 a[164:167], v10 offset:0\n"
    "ds_read_b128 a[180:183], v10 offset:1024\n"
    "ds_read_b128 a[168:171], v11 offset:0\n"
    "ds_read_b128 a[184:187], v11 offset:1024\n"
    "ds_read_b128 a[172:175], v12 offset:0\n"
    "ds_read_b128 a[188:191], v12 offset:1024\n"
    "v_mul_f32 v128, s72, v128\n"
    "v_mul_f32 v129, s72, v129\n"
    "v_mul_f32 v130, s72, v130\n"
    "v_mul_f32 v131, s72, v131\n"
    "v_cvt_pk_bf16_f32 v128, v128, v129\n"
    "v_cvt_pk_bf16_f32 v129, v130, v131\n"
    "ds_write_b64 v113, v[128:129] offset:30720\n"
    "v_mul_f32 v132, s72, v132\n"
    "v_mul_f32 v133, s72, v133\n"
    "v_mul_f32 v134, s72, v134\n"
    "v_mul_f32 v135, s72, v135\n"
    "v_cvt_pk_bf16_f32 v132, v132, v133\n"
    "v_cvt_pk_bf16_f32 v133, v134, v135\n"
    "ds_write_b64 v113, v[132:133] offset:32768\n"
    "v_mul_f32 v136, s72, v136\n"
    "v_mul_f32 v137, s72, v137\n"
    "v_mul_f32 v138, s72, v138\n"
    "v_mul_f32 v139, s72, v139\n"
    "v_cvt_pk_bf16_f32 v136, v136, v137\n"
    "v_cvt_pk_bf16_f32 v137, v138, v139\n"
    "ds_write_b64 v113, v[136:137] offset:34816\n"
    "v_mul_f32 v140, s72, v140\n"
    "v_mul_f32 v141, s72, v141\n"
    "v_mul_f32 v142, s72, v142\n"
    "v_mul_f32 v143, s72, v143\n"
    "v_cvt_pk_bf16_f32 v140, v140, v141\n"
    "v_cvt_pk_bf16_f32 v141, v142, v143\n"
    "ds_write_b64 v113, v[140:141] offset:36864\n"
    "s_waitcnt lgkmcnt(4)\n"
    "v_mul_f32 v144, s72, v144\n"
    "v_mul_f32 v145, s72, v145\n"
    "v_mul_f32 v146, s72, v146\n"
    "v_mul_f32 v147, s72, v147\n"
    "v_cvt_pk_bf16_f32 v144, v144, v145\n"
    "v_cvt_pk_bf16_f32 v145, v146, v147\n"
    "ds_write_b64 v113, v[144:145] offset:38912\n"
    "s_add_u32 s60, s69, 1\n"
    "s_and_b32 s60, s60, 15\n"
    "s_lshl_b32 s60, s60, 13\n"
    "s_add_u32 s61, s60, 4096\n"
    "s_add_u32 m0, s56, 0\n"
    "s_nop 0\n"
    "buffer_load_dwordx4 v7, s[40:43], s60 offen offset:0 nt lds\n"
    "v_mul_f32 v148, s72, v148\n"
    "v_mul_f32 v149, s72, v149\n"
    "v_mul_f32 v150, s72, v150\n"
    "v_mul_f32 v151, s72, v151\n"
    "v_cvt_pk_bf16_f32 v148, v148, v149\n"
    "v_cvt_pk_bf16_f32 v149, v150, v151\n"
    "ds_write_b64 v113, v[148:149] offset:31232\n"
    "v_mul_f32 v152, s72, v152\n"
    "v_mul_f32 v153, s72, v153\n"
    "v_mul_f32 v154, s72, v154\n"
    "v_mul_f32 v155, s72, v155\n"
    "v_cvt_pk_bf16_f32 v152, v152, v153\n"
    "v_cvt_pk_bf16_f32 v153, v154, v155\n"
    "ds_write_b64 v113, v[152:153] offset:33280\n"
    "buffer_load_dwordx4 v7, s[40:43], s60 offen offset:1024 nt lds\n"
    "v_mul_f32 v156, s72, v156\n"
    "v_mul_f32 v157, s72, v157\n"
    "v_mul_f32 v158, s72, v158\n"
    "v_mul_f32 v159, s72, v159\n"
    "v_cvt_pk_bf16_f32 v156, v156, v157\n"
    "v_cvt_pk_bf16_f32 v157, v158, v159\n"
    "ds_write_b64 v113, v[156:157] offset:35328\n"
    "v_mul_f32 v160, s72, v160\n"
    "v_mul_f32 v161, s72, v161\n"
    "v_mul_f32 v162, s72, v162\n"
    "v_mul_f32 v163, s72, v163\n"
    "v_cvt_pk_bf16_f32 v160, v160, v161\n"
    "v_cvt_pk_bf16_f32 v161, v162, v163\n"
    "ds_write_b64 v113, v[160:161] offset:37376\n"
    "buffer_load_dwordx4 v8, s[40:43], s60 offen offset:2048 nt lds\n"
    "v_mul_f32 v164, s72, v164\n"
    "v_mul_f32 v165, s72, v165\n"
    "v_mul_f32 v166, s72, v166\n"
    "v_mul_f32 v167, s72, v167\n"
    "v_cvt_pk_bf16_f32 v164, v164, v165\n"
    "v_cvt_pk_bf16_f32 v165, v166, v167\n"
    "ds_write_b64 v113, v[164:165] offset:39424\n"
    "v_mul_f32 v168, s72, v168\n"
    "v_mul_f32 v169, s72, v169\n"
    "v_mul_f32 v170, s72, v170\n"
    "v_mul_f32 v171, s72, v171\n"
    "v_cvt_pk_bf16_f32 v168, v168, v169\n"
    "v_cvt_pk_bf16_f32 v169, v170, v171\n"
    "ds_write_b64 v113, v[168:169] offset:31744\n"
    "buffer_load_dwordx4 v8, s[40:43], s60 offen offset:3072 nt lds\n"
    "v_mul_f32 v172, s72, v172\n"
    "v_mul_f32 v173, s72, v173\n"
    "v_mul_f32 v174, s72, v174\n"
    "v_mul_f32 v175, s72, v175\n"
    "v_cvt_pk_bf16_f32 v172, v172, v173\n"
    "v_cvt_pk_bf16_f32 v173, v174, v175\n"
    "ds_write_b64 v113, v[172:173] offset:33792\n"
    "v_mul_f32 v176, s72, v176\n"
    "v_mul_f32 v177, s72, v177\n"
    "v_mul_f32 v178, s72, v178\n"
    "v_mul_f32 v179, s72, v179\n"
    "v_cvt_pk_bf16_f32 v176, v176, v177\n"
    "v_cvt_pk_bf16_f32 v177, v178, v179\n"
    "ds_write_b64 v113, v[176:177] offset:35840\n"
    "s_add_u32 m0, s56, 4096\n"
    "s_nop 0\n"
    "buffer_load_dwordx4 v7, s[40:43], s61 offen offset:0 nt lds\n"
    "v_mul_f32 v180, s72, v180\n"
    "v_mul_f32 v181, s72, v181\n"
    "v_mul_f32 v182, s72, v182\n"
    "v_mul_f32 v183, s72, v183\n"
    "v_cvt_pk_bf16_f32 v180, v180, v181\n"
    "v_cvt_pk_bf16_f32 v181, v182, v183\n"
    "ds_write_b64 v113, v[180:181] offset:37888\n"
    "v_mul_f32 v184, s72, v184\n"
    "v_mul_f32 v185, s72, v185\n"
    "v_mul_f32 v186, s72, v186\n"
    "v_mul_f32 v187, s72, v187\n"
    "v_cvt_pk_bf16_f32 v184, v184, v185\n"
    "v_cvt_pk_bf16_f32 v185, v186, v187\n"
    "ds_write_b64 v113, v[184:185] offset:39936\n"
    "buffer_load_dwordx4 v7, s[40:43], s61 offen offset:1024 nt lds\n"
    "v_mul_f32 v188, s72, v188\n"
    "v_mul_f32 v189, s72, v189\n"
    "v_mul_f32 v190, s72, v190\n"
    "v_mul_f32 v191, s72, v191\n"
    "v_cvt_pk_bf16_f32 v188, v188, v189\n"
    "v_cvt_pk_bf16_f32 v189, v190, v191\n"
    "ds_write_b64 v113, v[188:189] offset:32256\n"
    "v_mul_f32 v192, s72, v192\n"
    "v_mul_f32 v193, s72, v193\n"
    "v_mul_f32 v194, s72, v194\n"
    "v_mul_f32 v195, s72, v195\n"
    "v_cvt_pk_bf16_f32 v192, v192, v193\n"
    "v_cvt_pk_bf16_f32 v193, v194, v195\n"
    "ds_write_b64 v113, v[192:193] offset:34304\n"
    "buffer_load_dwordx4 v8, s[40:43], s61 offen offset:2048 nt lds\n"
    "v_mul_f32 v196, s72, v196\n"
    "v_mul_f32 v197, s72, v197\n"
    "v_mul_f32 v198, s72, v198\n"
    "v_mul_f32 v199, s72, v199\n"
    "v_cvt_pk_bf16_f32 v196, v196, v197\n"
    "v_cvt_pk_bf16_f32 v197, v198, v199\n"
    "ds_write_b64 v113, v[196:197] offset:36352\n"
    "v_mul_f32 v200, s72, v200\n"
    "v_mul_f32 v201, s72, v201\n"
    "v_mul_f32 v202, s72, v202\n"
    "v_mul_f32 v203, s72, v203\n"
    "v_cvt_pk_bf16_f32 v200, v200, v201\n"
    "v_cvt_pk_bf16_f32 v201, v202, v203\n"
    "ds_write_b64 v113, v[200:201] offset:38400\n"
    "buffer_load_dwordx4 v8, s[40:43], s61 offen offset:3072 nt lds\n"
    "v_mul_f32 v204, s72, v204\n"
    "v_mul_f32 v205, s72, v205\n"
    "v_mul_f32 v206, s72, v206\n"
    "v_mul_f32 v207, s72, v207\n"
    "v_cvt_pk_bf16_f32 v204, v204, v205\n"
    "v_cvt_pk_bf16_f32 v205, v206, v207\n"
    "ds_write_b64 v113, v[204:205] offset:40448\n"
    "s_waitcnt vmcnt(16)\n"
    "ds_read_b64_tr_b16 a[192:193], v13 offset:0\n"
    "ds_read_b64_tr_b16 a[194:195], v13 offset:1024\n"
    "ds_read_b64_tr_b16 a[196:197], v14 offset:0\n"
    "ds_read_b64_tr_b16 a[198:199], v14 offset:1024\n"
    "ds_read_b64_tr_b16 a[200:201], v15 offset:0\n"
    "ds_read_b64_tr_b16 a[202:203], v15 offset:1024\n"
    "ds_read_b64_tr_b16 a[204:205], v16 offset:0\n"
    "ds_read_b64_tr_b16 a[206:207], v16 offset:1024\n"
    "ds_read_b64_tr_b16 a[208:209], v17 offset:0\n"
    "ds_read_b64_tr_b16 a[210:211], v17 offset:1024\n"
    "ds_read_b64_tr_b16 a[212:213], v18 offset:0\n"
    "ds_read_b64_tr_b16 a[214:215], v18 offset:1024\n"
    "ds_read_b64_tr_b16 a[216:217], v19 offset:0\n"
    "ds_read_b64_tr_b16 a[218:219], v19 offset:1024\n"
    "ds_read_b64_tr_b16 a[220:221], v20 offset:0\n"
    "ds_read_b64_tr_b16 a[222:223], v20 offset:1024\n"
    "s_add_u32 s75, s56, 24576\n"
    "s_mov_b32 s77, 0x3000c\n"
    "s_mov_b32 s70, 1\n"
    "s_waitcnt lgkmcnt(0)\n"
    "s_barrier\n"
    "ds_read_b128 a[80:83], v111 offset:30720\n"
    "ds_read_b128 a[84:87], v111 offset:31744\n"
    "ds_read_b128 a[88:91], v112 offset:30720\n"
    "ds_read_b128 a[92:95], v112 offset:31744\n"
    "ds_read_b128 a[96:99], v111 offset:32768\n"
    "ds_read_b128 a[100:103], v111 offset:33792\n"
    "ds_read_b128 a[104:107], v112 offset:32768\n"
    "ds_read_b128 a[108:111], v112 offset:33792\n"
    "ds_read_b128 a[112:115], v111 offset:34816\n"
    "ds_read_b128 a[116:119], v111 offset:35840\n"
    "ds_read_b128 a[120:123], v112 offset:34816\n"
    "ds_read_b128 a[124:127], v112 offset:35840\n"
    "ds_read_b128 a[128:131], v111 offset:36864\n"
    "ds_read_b128 a[132:135], v111 offset:37888\n"
    "ds_read_b128 a[136:139], v112 offset:36864\n"
    "ds_read_b128 a[140:143], v112 offset:37888\n"
    "ds_read_b128 a[144:147], v111 offset:38912\n"
    "ds_read_b128 a[148:151], v111 offset:39936\n"
    "ds_read_b128 a[152:155], v112 offset:38912\n"
    "ds_read_b128 a[156:159], v112 offset:39936\n"
    "v_mfma_f32_32x32x16_bf16 v[128:143], v[104:107], v[104:107], 0\n"
    "v_mfma_f32_32x32x16_bf16 v[144:159], v[104:107], v[104:107], 0\n"
    "v_mfma_f32_32x32x16_bf16 v[160:175], v[104:107], v[104:107], 0\n"
    "v_mfma_f32_32x32x16_bf16 v[176:191], v[104:107], v[104:107], 0\n"
    "v_mfma_f32_32x32x16_bf16 v[192:207], v[104:107], v[104:107], 0\n"
    "s_add_u32 s60, s69, 2\n"
    "s_and_b32 s60, s60, 15\n"
    "s_lshl_b32 s60, s60, 13\n"
    "s_add_u32 s61, s60, 4096\n"
    "s_add_u32 m0, s56, 8192\n"
    "s_nop 0\n"
    "buffer_load_dwordx4 v3, s[36:39], s60 offen offset:0 nt lds\n"
    "buffer_load_dwordx4 v3, s[36:39], s60 offen offset:1024 nt lds\n"
    "buffer_load_dwordx4 v4, s[36:39], s60 offen offset:2048 nt lds\n"
    "buffer_load_dwordx4 v4, s[36:39], s60 offen offset:3072 nt lds\n"
    "s_add_u32 m0, s56, 12288\n"
    "s_nop 0\n"
    "buffer_load_dwordx4 v5, s[36:39], s61 offen offset:0 nt lds\n"
    "buffer_load_dwordx4 v5, s[36:39], s61 offen offset:1024 nt lds\n"
    "buffer_load_dwordx4 v6, s[36:39], s61 offen offset:2048 nt lds\n"
    "buffer_load_dwordx4 v6, s[36:39], s61 offen offset:3072 nt lds\n"
    "v_mov_b32 v104, 0x3f803f80\n"
    "v_mov_b32 v105, 0x3f803f80\n"
    "v_mov_b32 v106, 0x3f803f80\n"
    "v_mov_b32 v107, 0x3f803f80\n"
    "s_branch LSLOW%=\n"
    "LPREA%=:\n"
    "v_mul_f32 v128, s72, v128\n"
    "v_mul_f32 v129, s72, v129\n"
    "v_mul_f32 v130, s72, v130\n"
    "v_mul_f32 v131, s72, v131\n"
    "v_cvt_pk_bf16_f32 v128, v128, v129\n"
    "v_cvt_pk_bf16_f32 v129, v130, v131\n"
    "ds_write_b64 v113, v[128:129] offset:30720\n"
    "v_mul_f32 v132, s72, v132\n"
    "v_mul_f32 v133, s72, v133\n"
    "v_mul_f32 v134, s72, v134\n"
    "v_mul_f32 v135, s72, v135\n"
    "v_cvt_pk_bf16_f32 v132, v132, v133\n"
    "v_cvt_pk_bf16_f32 v133, v134, v135\n"
    "ds_write_b64 v113, v[132:133] offset:32768\n"
    "v_mul_f32 v136, s72, v136\n"
    "v_mul_f32 v137, s72, v137\n"
    "v_mul_f32 v138, s72, v138\n"
    "v_mul_f32 v139, s72, v139\n"
    "v_cvt_pk_bf16_f32 v136, v136, v137\n"
    "v_cvt_pk_bf16_f32 v137, v138, v139\n"
    "ds_write_b64 v113, v[136:137] offset:34816\n"
    "v_mul_f32 v140, s72, v140\n"
    "v_mul_f32 v141, s72, v141\n"
    "v_mul_f32 v142, s72, v142\n"
    "v_mul_f32 v143, s72, v143\n"
    "v_cvt_pk_bf16_f32 v140, v140, v141\n"
    "v_cvt_pk_bf16_f32 v141, v142, v143\n"
    "ds_write_b64 v113, v[140:141] offset:36864\n"
    "v_mul_f32 v144, s72, v144\n"
    "v_mul_f32 v145, s72, v145\n"
    "v_mul_f32 v146, s72, v146\n"
    "v_mul_f32 v147, s72, v147\n"
    "v_cvt_pk_bf16_f32 v144, v144, v145\n"
    "v_cvt_pk_bf16_f32 v145, v146, v147\n"
    "ds_write_b64 v113, v[144:145] offset:38912\n"
    "v_mul_f32 v148, s72, v148\n"
    "v_mul_f32 v149, s72, v149\n"
    "v_mul_f32 v150, s72, v150\n"
    "v_mul_f32 v151, s72, v151\n"
    "v_cvt_pk_bf16_f32 v148, v148, v149\n"
    "v_cvt_pk_bf16_f32 v149, v150, v151\n"
    "ds_write_b64 v113, v[148:149] offset:31232\n"
    "v_mul_f32 v152, s72, v152\n"
    "v_mul_f32 v153, s72, v153\n"
    "v_mul_f32 v154, s72, v154\n"
    "v_mul_f32 v155, s72, v155\n"
    "v_cvt_pk_bf16_f32 v152, v152, v153\n"
    "v_cvt_pk_bf16_f32 v153, v154, v155\n"
    "ds_write_b64 v113, v[152:153] offset:33280\n"
    "v_mul_f32 v156, s72, v156\n"
    "v_mul_f32 v157, s72, v157\n"
    "v_mul_f32 v158, s72, v158\n"
    "v_mul_f32 v159, s72, v159\n"
    "v_cvt_pk_bf16_f32 v156, v156, v157\n"
    "v_cvt_pk_bf16_f32 v157, v158, v159\n"
    "ds_write_b64 v113, v[156:157] offset:35328\n"
    "v_mul_f32 v160, s72, v160\n"
    "v_mul_f32 v161, s72, v161\n"
    "v_mul_f32 v162, s72, v162\n"
    "v_mul_f32 v163, s72, v163\n"
    "v_cvt_pk_bf16_f32 v160, v160, v161\n"
    "v_cvt_pk_bf16_f32 v161, v162, v163\n"
    "ds_write_b64 v113, v[160:161] offset:37376\n"
    "v_mul_f32 v164, s72, v164\n"
    "v_mul_f32 v165, s72, v165\n"
    "v_mul_f32 v166, s72, v166\n"
    "v_mul_f32 v167, s72, v167\n"
    "v_cvt_pk_bf16_f32 v164, v164, v165\n"
    "v_cvt_pk_bf16_f32 v165, v166, v167\n"
    "ds_write_b64 v113, v[164:165] offset:39424\n"
    "v_mul_f32 v168, s72, v168\n"
    "v_mul_f32 v169, s72, v169\n"
    "v_mul_f32 v170, s72, v170\n"
    "v_mul_f32 v171, s72, v171\n"
    "v_cvt_pk_bf16_f32 v168, v168, v169\n"
    "v_cvt_pk_bf16_f32 v169, v170, v171\n"
    "ds_write_b64 v113, v[168:169] offset:31744\n"
    "v_mul_f32 v172, s72, v172\n"
    "v_mul_f32 v173, s72, v173\n"
    "v_mul_f32 v174, s72, v174\n"
    "v_mul_f32 v175, s72, v175\n"
    "v_cvt_pk_bf16_f32 v172, v172, v173\n"
    "v_cvt_pk_bf16_f32 v173, v174, v175\n"
    "ds_write_b64 v113, v[172:173] offset:33792\n"
    "v_mul_f32 v176, s72, v176\n"
    "v_mul_f32 v177, s72, v177\n"
    "v_mul_f32 v178, s72, v178\n"
    "v_mul_f32 v179, s72, v179\n"
    "v_cvt_pk_bf16_f32 v176, v176, v177\n"
    "v_cvt_pk_bf16_f32 v177, v178, v179\n"
    "ds_write_b64 v113, v[176:177] offset:35840\n"
    "v_mul_f32 v180, s72, v180\n"
    "v_mul_f32 v181, s72, v181\n"
    "v_mul_f32 v182, s72, v182\n"
    "v_mul_f32 v183, s72, v183\n"
    "v_cvt_pk_bf16_f32 v180, v180, v181\n"
    "v_cvt_pk_bf16_f32 v181, v182, v183\n"
    "ds_write_b64 v113, v[180:181] offset:37888\n"
    "v_mul_f32 v184, s72, v184\n"
    "v_mul_f32 v185, s72, v185\n"
    "v_mul_f32 v186, s72, v186\n"
    "v_mul_f32 v187, s72, v187\n"
    "v_cvt_pk_bf16_f32 v184, v184, v185\n"
    "v_cvt_pk_bf16_f32 v185, v186, v187\n"
    "ds_write_b64 v113, v[184:185] offset:39936\n"
    "v_mul_f32 v188, s72, v188\n"
    "v_mul_f32 v189, s72, v189\n"
    "v_mul_f32 v190, s72, v190\n"
    "v_mul_f32 v191, s72, v191\n"
    "v_cvt_pk_bf16_f32 v188, v188, v189\n"
    "v_cvt_pk_bf16_f32 v189, v190, v191\n"
    "ds_write_b64 v113, v[188:189] offset:32256\n"
    "v_mul_f32 v192, s72, v192\n"
    "v_mul_f32 v193, s72, v193\n"
    "v_mul_f32 v194, s72, v194\n"
    "v_mul_f32 v195, s72, v195\n"
    "v_cvt_pk_bf16_f32 v192, v192, v193\n"
    "v_cvt_pk_bf16_f32 v193, v194, v195\n"
    "ds_write_b64 v113, v[192:193] offset:34304\n"
    "v_mul_f32 v196, s72, v196\n"
    "v_mul_f32 v197, s72, v197\n"
    "v_mul_f32 v198, s72, v198\n"
    "v_mul_f32 v199, s72, v199\n"
    "v_cvt_pk_bf16_f32 v196, v196, v197\n"
    "v_cvt_pk_bf16_f32 v197, v198, v199\n"
    "ds_write_b64 v113, v[196:197] offset:36352\n"
    "v_mul_f32 v200, s72, v200\n"
    "v_mul_f32 v201, s72, v201\n"
    "v_mul_f32 v202, s72, v202\n"
    "v_mul_f32 v203, s72, v203\n"
    "v_cvt_pk_bf16_f32 v200, v200, v201\n"
    "v_cvt_pk_bf16_f32 v201, v202, v203\n"
    "ds_write_b64 v113, v[200:201] offset:38400\n"
    "v_mul_f32 v204, s72, v204\n"
    "v_mul_f32 v205, s72, v205\n"
    "v_mul_f32 v206, s72, v206\n"
    "v_mul_f32 v207, s72, v207\n"
    "v_cvt_pk_bf16_f32 v204, v204, v205\n"
    "v_cvt_pk_bf16_f32 v205, v206, v207\n"
    "ds_write_b64 v113, v[204:205] offset:40448\n"
    "s_waitcnt lgkmcnt(0)\n"
    "s_barrier\n"
    "ds_read_b128 a[80:83], v111 offset:30720\n"
    "ds_read_b128 a[84:87], v111 offset:31744\n"
    "ds_read_b128 a[88:91], v112 offset:30720\n"
    "ds_read_b128 a[92:95], v112 offset:31744\n"
    "ds_read_b128 a[96:99], v111 offset:32768\n"
    "ds_read_b128 a[100:103], v111 offset:33792\n"
    "ds_read_b128 a[104:107], v112 offset:32768\n"
    "ds_read_b128 a[108:111], v112 offset:33792\n"
    "ds_read_b128 a[112:115], v111 offset:34816\n"
    "ds_read_b128 a[116:119], v111 offset:35840\n"
    "ds_read_b128 a[120:123], v112 offset:34816\n"
    "ds_read_b128 a[124:127], v112 offset:35840\n"
    "ds_read_b128 a[128:131], v111 offset:36864\n"
    "ds_read_b128 a[132:135], v111 offset:37888\n"
    "ds_read_b128 a[136:139], v112 offset:36864\n"
    "ds_read_b128 a[140:143], v112 offset:37888\n"
    "ds_read_b128 a[144:147], v111 offset:38912\n"
    "ds_read_b128 a[148:151], v111 offset:39936\n"
    "ds_read_b128 a[152:155], v112 offset:38912\n"
    "ds_read_b128 a[156:159], v112 offset:39936\n"
    "v_mfma_f32_32x32x16_bf16 v[128:143], v[104:107], v[104:107], 0\n"
    "v_mfma_f32_32x32x16_bf16 v[144:159], v[104:107], v[104:107], 0\n"
    "v_mfma_f32_32x32x16_bf16 v[160:175], v[104:107], v[104:107], 0\n"
    "v_mfma_f32_32x32x16_bf16 v[176:191], v[104:107], v[104:107], 0\n"
    "v_mfma_f32_32x32x16_bf16 v[192:207], v[104:107], v[104:107], 0\n"
    "s_waitcnt vmcnt(16) lgkmcnt(0)\n"
    "s_barrier\n"
    "v_mov_b32 v104, 0x3f803f80\n"
    "v_mov_b32 v105, 0x3f803f80\n"
    "v_mov_b32 v106, 0x3f803f80\n"
    "v_mov_b32 v107, 0x3f803f80\n"
    "s_mul_i32 s64, s30, 40960\n"
    "s_add_u32 s58, s68, s64\n"
    "s_add_u32 s59, s58, 81920\n"
    "s_add_u32 s75, s58, 40960\n"
    "s_add_u32 s76, s59, 40960\n"
    "s_add_u32 s77, s56, 40960\n"
    "v_add_u32 v9, s58, v1\n"
    "v_xor_b32 v10, 0x40, v9\n"
    "v_xor_b32 v11, 0x80, v9\n"
    "v_xor_b32 v12, 0xc0, v9\n"
    "ds_read_b128 a[160:163], v9 offset:0\n"
    "ds_read_b128 a[176:179], v9 offset:1024\n"
    "ds_read_b128 a[164:167], v10 offset:0\n"
    "ds_read_b128 a[180:183], v10 offset:1024\n"
    "ds_read_b128 a[168:171], v11 offset:0\n"
    "ds_read_b128 a[184:187], v11 offset:1024\n"
    "ds_read_b128 a[172:175], v12 offset:0\n"
    "ds_read_b128 a[188:191], v12 offset:1024\n"
    "s_add_u32 s60, s69, 3\n"
    "s_and_b32 s60, s60, 15\n"
    "s_lshl_b32 s60, s60, 13\n"
    "s_add_u32 s61, s60, 4096\n"
    "s_add_u32 m0, s56, 24576\n"
    "s_nop 0\n"
    "buffer_load_dwordx4 v3, s[36:39], s60 offen offset:0 nt lds\n"
    "buffer_load_dwordx4 v3, s[36:39], s60 offen offset:1024 nt lds\n"
    "buffer_load_dwordx4 v4, s[36:39], s60 offen offset:2048 nt lds\n"
    "buffer_load_dwordx4 v4, s[36:39], s60 offen offset:3072 nt lds\n"
    "s_add_u32 m0, s56, 28672\n"
    "s_nop 0\n"
    "buffer_load_dwordx4 v5, s[36:39], s61 offen offset:0 nt lds\n"
    "buffer_load_dwordx4 v5, s[36:39], s61 offen offset:1024 nt lds\n"
    "buffer_load_dwordx4 v6, s[36:39], s61 offen offset:2048 nt lds\n"
    "buffer_load_dwordx4 v6, s[36:39], s61 offen offset:3072 nt lds\n"
    "s_add_u32 s60, s69, 4\n"
    "s_and_b32 s60, s60, 15\n"
    "s_lshl_b32 s60, s60, 13\n"
    "s_add_u32 s61, s60, 4096\n"
    "s_cmp_eq_u32 s31, 0\n"
    "s_cselect_b32 s60, s60, 0x7ff00000\n"
    "s_add_u32 s61, s60, 4096\n"
    "s_add_u32 m0, s56, 32768\n"
    "s_nop 0\n"
    "buffer_load_dwordx4 v3, s[36:39], s60 offen offset:0 nt lds\n"
    "buffer_load_dwordx4 v3, s[36:39], s60 offen offset:1024 nt lds\n"
    "buffer_load_dwordx4 v4, s[36:39], s60 offen offset:2048 nt lds\n"
    "buffer_load_dwordx4 v4, s[36:39], s60 offen offset:3072 nt lds\n"
    "s_add_u32 m0, s56, 36864\n"
    "s_nop 0\n"
    "buffer_load_dwordx4 v5, s[36:39], s61 offen offset:0 nt lds\n"
    "buffer_load_dwordx4 v5, s[36:39], s61 offen offset:1024 nt lds\n"
    "buffer_load_dwordx4 v6, s[36:39], s61 offen offset:2048 nt lds\n"
    "buffer_load_dwordx4 v6, s[36:39], s61 offen offset:3072 nt lds\n"
    "s_add_u32 s58, s58, 8192\n"
    "s_sub_u32 s70, 5, s31\n"
    "s_mul_i32 s64, s31, 32768\n"
    "s_add_u32 s71, s56, s64\n"
    "s_add_u32 s60, s69, s70\n"
    "s_and_b32 s60, s60, 15\n"
    "s_lshl_b32 s60, s60, 13\n"
    "s_cmp_lt_u32 s70, 16\n"
    "s_cselect_b32 s60, s60, 0x7ff00000\n"
    "s_add_u32 s61, s60, 4096\n"
    "v_add_u32 v13, s59, v2\n"
    "v_xor_b32 v14, 0x20, v13\n"
    "v_xor_b32 v15, 0x40, v13\n"
    "v_xor_b32 v16, 0x60, v13\n"
    "v_xor_b32 v17, 0x80, v13\n"
    "v_xor_b32 v18, 0xa0, v13\n"
    "v_xor_b32 v19, 0xc0, v13\n"
    "v_xor_b32 v20, 0xe0, v13\n"
    "v_add_u32 v9, s58, v1\n"
    "v_xor_b32 v10, 0x40, v9\n"
    "v_xor_b32 v11, 0x80, v9\n"
    "v_xor_b32 v12, 0xc0, v9\n"
    "s_waitcnt vmcnt(24) lgkmcnt(0)\n"
    "s_barrier\n"
    "ds_read_b64_tr_b16 a[192:193], v13 offset:0\n"
    "ds_read_b64_tr_b16 a[194:195], v13 offset:1024\n"
    "ds_read_b64_tr_b16 a[196:197], v14 offset:0\n"
    "ds_read_b64_tr_b16 a[198:199], v14 offset:1024\n"
    "ds_read_b64_tr_b16 a[200:201], v15 offset:0\n"
    "ds_read_b64_tr_b16 a[202:203], v15 offset:1024\n"
    "ds_read_b64_tr_b16 a[204:205], v16 offset:0\n"
    "ds_read_b64_tr_b16 a[206:207], v16 offset:1024\n"
    "ds_read_b64_tr_b16 a[208:209], v17 offset:0\n"
    "ds_read_b64_tr_b16 a[210:211], v17 offset:1024\n"
    "ds_read_b64_tr_b16 a[212:213], v18 offset:0\n"
    "ds_read_b64_tr_b16 a[214:215], v18 offset:1024\n"
    "ds_read_b64_tr_b16 a[216:217], v19 offset:0\n"
    "ds_read_b64_tr_b16 a[218:219], v19 offset:1024\n"
    "ds_read_b64_tr_b16 a[220:221], v20 offset:0\n"
    "ds_read_b64_tr_b16 a[222:223], v20 offset:1024\n"
    "s_branch LSLOW%=\n"
    ".p2align 6\n"
    "LLOOPB%=:\n"
    "s_waitcnt lgkmcnt(0)\n"
    "v_mfma_f32_16x16x32_bf16 v[64:67], a[160:163], a[80:83], v[24:27]\n"
    "s_mov_b32 m0, s24\n"
    "v_mfma_f32_16x16x32_bf16 v[68:71], a[176:179], a[80:83], v[24:27]\n"
    "buffer_load_dwordx4 v7, s[40:43], s60 offen offset:0 nt lds\n"
    "v_mfma_f32_16x16x32_bf16 v[64:67], a[164:167], a[84:87], v[64:67]\n"
    "buffer_load_dwordx4 v7, s[40:43], s60 offen offset:1024 nt lds\n"
    "v_mfma_f32_16x16x32_bf16 v[68:71], a[180:183], a[84:87], v[68:71]\n"
    "v_mfma_f32_16x16x32_bf16 v[64:67], a[168:171], a[88:91], v[64:67]\n"
    "v_mfma_f32_16x16x32_bf16 v[68:71], a[184:187], a[88:91], v[68:71]\n"
    "v_mfma_f32_16x16x32_bf16 v[64:67], a[172:175], a[92:95], v[64:67]\n"
    "v_mfma_f32_16x16x32_bf16 v[68:71], a[188:191], a[92:95], v[68:71]\n"
    "buffer_load_dwordx4 v8, s[40:43], s60 offen offset:2048 nt lds\n"
    "v_mfma_f32_16x16x32_bf16 v[72:75], a[160:163], a[96:99], v[28:31]\n"
    "v_mfma_f32_16x16x32_bf16 v[76:79], a[176:179], a[96:99], v[28:31]\n"
    "v_mfma_f32_16x16x32_bf16 v[72:75], a[164:167], a[100:103], v[72:75]\n"
    "v_mfma_f32_16x16x32_bf16 v[76:79], a[180:183], a[100:103], v[76:79]\n"
    "buffer_load_dwordx4 v8, s[40:43], s60 offen offset:3072 nt lds\n"
    "s_add_u32 m0, s24, 4096\n"
    "v_max_f32 v21, v64, v65\n"
    "v_mfma_f32_16x16x32_bf16 v[72:75], a[168:171], a[104:107], v[72:75]\n"
    "v_max3_f32 v21, v21, v66, v67\n"
    "v_max3_f32 v21, v21, v68, v69\n"
    "v_max3_f32 v21, v21, v70, v71\n"
    "v_mfma_f32_16x16x32_bf16 v[76:79], a[184:187], a[104:107], v[76:79]\n"
    "v_exp_f32 v64, v64\n"
    "v_mfma_f32_16x16x32_bf16 v[72:75], a[172:175], a[108:111], v[72:75]\n"
    "v_exp_f32 v65, v65\n"
    "v_mfma_f32_16x16x32_bf16 v[76:79], a[188:191], a[108:111], v[76:79]\n"
    "v_exp_f32 v66, v66\n"
    "v_cvt_pk_bf16_f32 v44, v64, v65\n"
    "v_mfma_f32_16x16x32_bf16 v[80:83], a[160:163], a[112:115], v[32:35]\n"
    "buffer_load_dwordx4 v7, s[40:43], s61 offen offset:0 nt lds\n"
    "v_exp_f32 v67, v67\n"
    "v_mfma_f32_16x16x32_bf16 v[84:87], a[176:179], a[112:115], v[32:35]\n"
    "v_max3_f32 v21, v21, v72, v73\n"
    "v_max3_f32 v21, v21, v74, v75\n"
    "v_max3_f32 v21, v21, v76, v77\n"
    "v_mfma_f32_16x16x32_bf16 v[80:83], a[164:167], a[116:119], v[80:83]\n"
    "v_max3_f32 v21, v21, v78, v79\n"
    "v_cvt_pk_bf16_f32 v45, v66, v67\n"
    "v_mfma_f32_16x16x32_bf16 v[84:87], a[180:183], a[116:119], v[84:87]\n"
    "v_exp_f32 v68, v68\n"
    "v_mfma_f32_16x16x32_bf16 v[80:83], a[168:171], a[120:123], v[80:83]\n"
    "v_exp_f32 v69, v69\n"
    "v_mfma_f32_16x16x32_bf16 v[84:87], a[184:187], a[120:123], v[84:87]\n"
    "v_exp_f32 v70, v70\n"
    "v_cvt_pk_bf16_f32 v46, v68, v69\n"
    "v_mfma_f32_16x16x32_bf16 v[80:83], a[172:175], a[124:127], v[80:83]\n"
    "v_exp_f32 v71, v71\n"
    "v_mfma_f32_16x16x32_bf16 v[84:87], a[188:191], a[124:127], v[84:87]\n"
    "s_waitcnt vmcnt(13)\n"
    "v_exp_f32 v72, v72\n"
    "v_cvt_pk_bf16_f32 v47, v70, v71\n"
    "v_mfma_f32_16x16x32_bf16 v[88:91], a[160:163], a[128:131], v[36:39]\n"
    "ds_read_b64_tr_b16 a[192:193], v13 offset:0\n"
    "v_exp_f32 v73, v73\n"
    "v_mfma_f32_16x16x32_bf16 v[92:95], a[176:179], a[128:131], v[36:39]\n"
    "ds_read_b64_tr_b16 a[194:195], v13 offset:1024\n"
    "ds_read_b64_tr_b16 a[196:197], v14 offset:0\n"
    "v_mfma_f32_16x16x32_bf16 v[88:91], a[164:167], a[132:135], v[88:91]\n"
    "ds_read_b64_tr_b16 a[198:199], v14 offset:1024\n"
    "v_max3_f32 v21, v21, v80, v81\n"
    "v_max3_f32 v21, v21, v82, v83\n"
    "v_mfma_f32_16x16x32_bf16 v[92:95], a[180:183], a[132:135], v[92:95]\n"
    "buffer_load_dwordx4 v7, s[40:43], s61 offen offset:1024 nt lds\n"
    "v_max3_f32 v21, v21, v84, v85\n"
    "v_max3_f32 v21, v21, v86, v87\n"
    "v_mfma_f32_16x16x32_bf16 v[88:91], a[168:171], a[136:139], v[88:91]\n"
    "ds_read_b64_tr_b16 a[200:201], v15 offset:0\n"
    "ds_read_b64_tr_b16 a[202:203], v15 offset:1024\n"
    "v_mfma_f32_16x16x32_bf16 v[92:95], a[184:187], a[136:139], v[92:95]\n"
    "ds_read_b64_tr_b16 a[204:205], v16 offset:0\n"
    "v_cvt_pk_bf16_f32 v48, v72, v73\n"
    "v_mfma_f32_16x16x32_bf16 v[88:91], a[172:175], a[140:143], v[88:91]\n"
    "ds_read_b64_tr_b16 a[206:207], v16 offset:1024\n"
    "v_exp_f32 v74, v74\n"
    "v_mfma_f32_16x16x32_bf16 v[92:95], a[188:191], a[140:143], v[92:95]\n"
    "ds_read_b64_tr_b16 a[208:209], v17 offset:0\n"
    "v_exp_f32 v75, v75\n"
    "v_mfma_f32_16x16x32_bf16 v[96:99], a[160:163], a[144:147], v[40:43]\n"
    "buffer_load_dwordx4 v8, s[40:43], s61 offen offset:2048 nt lds\n"
    "v_cvt_pk_bf16_f32 v49, v74, v75\n"
    "v_max3_f32 v21, v21, v88, v89\n"
    "v_mfma_f32_16x16x32_bf16 v[100:103], a[176:179], a[144:147], v[40:43]\n"
    "ds_read_b64_tr_b16 a[210:211], v17 offset:1024\n"
    "ds_read_b64_tr_b16 a[212:213], v18 offset:0\n"
    "v_mfma_f32_16x16x32_bf16 v[96:99], a[164:167], a[148:151], v[96:99]\n"
    "ds_read_b64_tr_b16 a[214:215], v18 offset:1024\n"
    "v_max3_f32 v21, v21, v90, v91\n"
    "v_max3_f32 v21, v21, v92, v93\n"
    "v_mfma_f32_16x16x32_bf16 v[100:103], a[180:183], a[148:151], v[100:103]\n"
    "ds_read_b64_tr_b16 a[216:217], v19 offset:0\n"
    "v_max3_f32 v21, v21, v94, v95\n"
    "v_cmp_lt_f32 vcc, s63, v21\n"
    "v_mfma_f32_16x16x32_bf16 v[96:99], a[168:171], a[152:155], v[96:99]\n"
    "ds_read_b64_tr_b16 a[218:219], v19 offset:1024\n"
    "v_exp_f32 v76, v76\n"
    "v_mfma_f32_16x16x32_bf16 v[100:103], a[184:187], a[152:155], v[100:103]\n"
    "buffer_load_dwordx4 v8, s[40:43], s61 offen offset:3072 nt lds\n"
    "v_exp_f32 v77, v77\n"
    "v_mfma_f32_16x16x32_bf16 v[96:99], a[172:175], a[156:159], v[96:99]\n"
    "ds_read_b64_tr_b16 a[220:221], v20 offset:0\n"
    "ds_read_b64_tr_b16 a[222:223], v20 offset:1024\n"
    "v_mfma_f32_16x16x32_bf16 v[100:103], a[188:191], a[156:159], v[100:103]\n"
    "v_exp_f32 v78, v78\n"
    "v_cvt_pk_bf16_f32 v50, v76, v77\n"
    "v_exp_f32 v79, v79\n"
    "s_nop 0\n"
    "v_cvt_pk_bf16_f32 v51, v78, v79\n"
    "s_cbranch_vccnz LSLOW%=\n"
    "LPHBB%=:\n"
    "s_add_u32 s70, s70, 1\n"
    "s_add_u32 s60, s69, s70\n"
    "s_and_b32 s60, s60, 15\n"
    "s_lshl_b32 s60, s60, 13\n"
    "s_cmp_lt_u32 s70, 16\n"
    "s_cselect_b32 s60, s60, 0x7ff00000\n"
    "s_add_u32 s61, s60, 4096\n"
    "s_mov_b32 s71, s59\n"
    "s_bitcmp1_b32 s77, s70\n"
    "s_cbranch_scc1 LSPEC%=\n"
    "LSPECX%=:\n"
    "s_waitcnt lgkmcnt(0)\n"
    "v_mfma_f32_16x16x32_bf16 v[128:131], a[192:195], v[44:47], v[128:131]\n"
    "s_mov_b32 m0, s71\n"
    "v_exp_f32 v80, v80\n"
    "v_mfma_f32_16x16x32_bf16 v[148:151], a[196:199], v[44:47], v[148:151]\n"
    "buffer_load_dwordx4 v3, s[36:39], s60 offen offset:0 nt lds\n"
    "v_max_f32 v22, v96, v97\n"
    "v_mfma_f32_16x16x32_bf16 v[168:171], a[200:203], v[44:47], v[168:171]\n"
    "s_add_u32 s58, s58, 16384\n"
    "v_exp_f32 v81, v81\n"
    "v_mfma_f32_16x16x32_bf16 v[188:191], a[204:207], v[44:47], v[188:191]\n"
    "buffer_load_dwordx4 v3, s[36:39], s60 offen offset:1024 nt lds\n"
    "v_cvt_pk_bf16_f32 v52, v80, v81\n"
    "v_mfma_f32_16x16x32_bf16 a[0:3], a[208:211], v[44:47], a[0:3]\n"
    "s_sub_u32 s64, s58, 24576\n"
    "v_exp_f32 v82, v82\n"
    "v_mfma_f32_16x16x32_bf16 a[20:23], a[212:215], v[44:47], a[20:23]\n"
    "s_add_u32 s59, s59, 16384\n"
    "v_exp_f32 v83, v83\n"
    "v_mfma_f32_16x16x32_bf16 a[40:43], a[216:219], v[44:47], a[40:43]\n"
    "s_add_u32 s62, s62, 1\n"
    "v_cvt_pk_bf16_f32 v53, v82, v83\n"
    "v_max3_f32 v22, v22, v98, v99\n"
    "v_mfma_f32_16x16x32_bf16 a[60:63], a[220:223], v[44:47], a[60:63]\n"
    "v_exp_f32 v84, v84\n"
    "v_mfma_f32_16x16x32_bf16 a[224:227], v[104:107], v[44:47], a[224:227]\n"
    "buffer_load_dwordx4 v4, s[36:39], s60 offen offset:2048 nt lds\n"
    "v_max3_f32 v22, v22, v100, v101\n"
    "v_mfma_f32_16x16x32_bf16 v[132:135], a[192:195], v[48:51], v[132:135]\n"
    "v_exp_f32 v85, v85\n"
    "v_mfma_f32_16x16x32_bf16 v[152:155], a[196:199], v[48:51], v[152:155]\n"
    "v_exp_f32 v86, v86\n"
    "v_mfma_f32_16x16x32_bf16 v[172:175], a[200:203], v[48:51], v[172:175]\n"
    "v_exp_f32 v87, v87\n"
    "v_mfma_f32_16x16x32_bf16 v[192:195], a[204:207], v[48:51], v[192:195]\n"
    "v_cvt_pk_bf16_f32 v54, v84, v85\n"
    "v_cvt_pk_bf16_f32 v55, v86, v87\n"
    "v_mfma_f32_16x16x32_bf16 a[4:7], a[208:211], v[48:51], a[4:7]\n"
    "buffer_load_dwordx4 v4, s[36:39], s60 offen offset:3072 nt lds\n"
    "v_max3_f32 v22, v22, v102, v103\n"
    "v_mfma_f32_16x16x32_bf16 a[24:27], a[212:215], v[48:51], a[24:27]\n"
    "s_add_u32 m0, s71, 4096\n"
    "v_exp_f32 v88, v88\n"
    "v_mfma_f32_16x16x32_bf16 a[44:47], a[216:219], v[48:51], a[44:47]\n"
    "v_exp_f32 v89, v89\n"
    "v_mfma_f32_16x16x32_bf16 a[64:67], a[220:223], v[48:51], a[64:67]\n"
    "v_cmp_lt_f32 vcc, s63, v22\n"
    "v_cvt_pk_bf16_f32 v56, v88, v89\n"
    "v_mfma_f32_16x16x32_bf16 a[228:231], v[104:107], v[48:51], a[228:231]\n"
    "s_cbranch_vccnz LS4B%=\n"
    "LS4XB%=:\n"
    "v_exp_f32 v90, v90\n"
    "v_mfma_f32_16x16x32_bf16 v[136:139], a[192:195], v[52:55], v[136:139]\n"
    "v_exp_f32 v91, v91\n"
    "v_mfma_f32_16x16x32_bf16 v[156:159], a[196:199], v[52:55], v[156:159]\n"
    "buffer_load_dwordx4 v5, s[36:39], s61 offen offset:0 nt lds\n"
    "v_cvt_pk_bf16_f32 v57, v90, v91\n"
    "v_mfma_f32_16x16x32_bf16 v[176:179], a[200:203], v[52:55], v[176:179]\n"
    "v_exp_f32 v92, v92\n"
    "v_mfma_f32_16x16x32_bf16 v[196:199], a[204:207], v[52:55], v[196:199]\n"
    "v_exp_f32 v93, v93\n"
    "v_mfma_f32_16x16x32_bf16 a[8:11], a[208:211], v[52:55], a[8:11]\n"
    "v_exp_f32 v94, v94\n"
    "v_mfma_f32_16x16x32_bf16 a[28:31], a[212:215], v[52:55], a[28:31]\n"
    "buffer_load_dwordx4 v5, s[36:39], s61 offen offset:1024 nt lds\n"
    "v_cvt_pk_bf16_f32 v58, v92, v93\n"
    "v_mfma_f32_16x16x32_bf16 a[48:51], a[216:219], v[52:55], a[48:51]\n"
    "v_exp_f32 v95, v95\n"
    "v_mfma_f32_16x16x32_bf16 a[68:71], a[220:223], v[52:55], a[68:71]\n"
    "s_cmp_ge_u32 s58, s75\n"
    "s_cselect_b32 s58, s64, s58\n"
    "v_cvt_pk_bf16_f32 v59, v94, v95\n"
    "v_mfma_f32_16x16x32_bf16 a[232:235], v[104:107], v[52:55], a[232:235]\n"
    "s_sub_u32 s64, s59, 24576\n"
    "v_exp_f32 v96, v96\n"
    "v_mfma_f32_16x16x32_bf16 v[140:143], a[192:195], v[56:59], v[140:143]\n"
    "v_exp_f32 v97, v97\n"
    "v_mfma_f32_16x16x32_bf16 v[160:163], a[196:199], v[56:59], v[160:163]\n"
    "buffer_load_dwordx4 v6, s[36:39], s61 offen offset:2048 nt lds\n"
    "v_cvt_pk_bf16_f32 v60, v96, v97\n"
    "v_mfma_f32_16x16x32_bf16 v[180:183], a[200:203], v[56:59], v[180:183]\n"
    "v_exp_f32 v98, v98\n"
    "v_exp_f32 v99, v99\n"
    "v_mfma_f32_16x16x32_bf16 v[200:203], a[204:207], v[56:59], v[200:203]\n"
    "v_cvt_pk_bf16_f32 v61, v98, v99\n"
    "v_add_u32 v9, s58, v1\n"
    "v_mfma_f32_16x16x32_bf16 a[12:15], a[208:211], v[56:59], a[12:15]\n"
    "v_exp_f32 v100, v100\n"
    "v_mfma_f32_16x16x32_bf16 a[32:35], a[212:215], v[56:59], a[32:35]\n"
    "v_exp_f32 v101, v101\n"
    "v_mfma_f32_16x16x32_bf16 a[52:55], a[216:219], v[56:59], a[52:55]\n"
    "v_exp_f32 v102, v102\n"
    "v_exp_f32 v103, v103\n"
    "v_mfma_f32_16x16x32_bf16 a[72:75], a[220:223], v[56:59], a[72:75]\n"
    "v_cvt_pk_bf16_f32 v62, v100, v101\n"
    "v_cvt_pk_bf16_f32 v63, v102, v103\n"
    "v_xor_b32 v10, 0x40, v9\n"
    "v_xor_b32 v11, 0x80, v9\n"
    "v_xor_b32 v12, 0xc0, v9\n"
    "v_mfma_f32_16x16x32_bf16 a[236:239], v[104:107], v[56:59], a[236:239]\n"
    "s_waitcnt vmcnt(15)\n"
    "s_cmp_ge_u32 s59, s75\n"
    "s_cselect_b32 s59, s64, s59\n"
    "v_mfma_f32_16x16x32_bf16 v[144:147], a[192:195], v[60:63], v[144:147]\n"
    "ds_read_b128 a[160:163], v9 offset:0\n"
    "v_add_u32 v13, s59, v2\n"
    "v_mfma_f32_16x16x32_bf16 v[164:167], a[196:199], v[60:63], v[164:167]\n"
    "ds_read_b128 a[176:179], v9 offset:1024\n"
    "v_xor_b32 v14, 0x20, v13\n"
    "v_mfma_f32_16x16x32_bf16 v[184:187], a[200:203], v[60:63], v[184:187]\n"
    "ds_read_b128 a[164:167], v10 offset:0\n"
    "v_xor_b32 v15, 0x40, v13\n"
    "v_mfma_f32_16x16x32_bf16 v[204:207], a[204:207], v[60:63], v[204:207]\n"
    "ds_read_b128 a[180:183], v10 offset:1024\n"
    "v_xor_b32 v16, 0x60, v13\n"
    "v_mfma_f32_16x16x32_bf16 a[16:19], a[208:211], v[60:63], a[16:19]\n"
    "buffer_load_dwordx4 v6, s[36:39], s61 offen offset:3072 nt lds\n"
    "ds_read_b128 a[168:171], v11 offset:0\n"
    "v_mfma_f32_16x16x32_bf16 a[36:39], a[212:215], v[60:63], a[36:39]\n"
    "ds_read_b128 a[184:187], v11 offset:1024\n"
    "v_xor_b32 v17, 0x80, v13\n"
    "v_mfma_f32_16x16x32_bf16 a[56:59], a[216:219], v[60:63], a[56:59]\n"
    "ds_read_b128 a[172:175], v12 offset:0\n"
    "v_xor_b32 v18, 0xa0, v13\n"
    "v_mfma_f32_16x16x32_bf16 a[76:79], a[220:223], v[60:63], a[76:79]\n"
    "s_cmp_eq_u32 s70, 16\n"
    "s_cselect_b32 s24, s24, s58\n"
    "ds_read_b128 a[188:191], v12 offset:1024\n"
    "v_mfma_f32_16x16x32_bf16 a[240:243], v[104:107], v[60:63], a[240:243]\n"
    "s_cmp_lt_u32 s62, 16\n"
    "v_xor_b32 v19, 0xc0, v13\n"
    "v_xor_b32 v20, 0xe0, v13\n"
    "s_cbranch_scc1 LLOOPB%=\n"
    "s_branch LEPIB%=\n"
    ".p2align 6\n"
    "LLOOPA%=:\n"
    "v_mfma_f32_16x16x32_bf16 v[64:67], a[160:163], a[80:83], v[24:27]\n"
    "ds_read_b64_tr_b16 a[192:193], v13 offset:0\n"
    "v_add_u32 v9, s58, v1\n"
    "v_mfma_f32_16x16x32_bf16 v[68:71], a[176:179], a[80:83], v[24:27]\n"
    "ds_read_b64_tr_b16 a[194:195], v13 offset:1024\n"
    "v_xor_b32 v10, 0x40, v9\n"
    "v_mfma_f32_16x16x32_bf16 v[64:67], a[164:167], a[84:87], v[64:67]\n"
    "ds_read_b64_tr_b16 a[196:197], v14 offset:0\n"
    "v_xor_b32 v11, 0x80, v9\n"
    "v_mfma_f32_16x16x32_bf16 v[68:71], a[180:183], a[84:87], v[68:71]\n"
    "ds_read_b64_tr_b16 a[198:199], v14 offset:1024\n"
    "v_xor_b32 v12, 0xc0, v9\n"
    "v_mfma_f32_16x16x32_bf16 v[64:67], a[168:171], a[88:91], v[64:67]\n"
    "ds_read_b64_tr_b16 a[200:201], v15 offset:0\n"
    "v_mfma_f32_16x16x32_bf16 v[68:71], a[184:187], a[88:91], v[68:71]\n"
    "ds_read_b64_tr_b16 a[202:203], v15 offset:1024\n"
    "v_mfma_f32_16x16x32_bf16 v[64:67], a[172:175], a[92:95], v[64:67]\n"
    "ds_read_b64_tr_b16 a[204:205], v16 offset:0\n"
    "v_mfma_f32_16x16x32_bf16 v[68:71], a[188:191], a[92:95], v[68:71]\n"
    "ds_read_b64_tr_b16 a[206:207], v16 offset:1024\n"
    "v_mfma_f32_16x16x32_bf16 v[72:75], a[160:163], a[96:99], v[28:31]\n"
    "ds_read_b64_tr_b16 a[208:209], v17 offset:0\n"
    "v_mfma_f32_16x16x32_bf16 v[76:79], a[176:179], a[96:99], v[28:31]\n"
    "ds_read_b64_tr_b16 a[210:211], v17 offset:1024\n"
    "v_mfma_f32_16x16x32_bf16 v[72:75], a[164:167], a[100:103], v[72:75]\n"
    "ds_read_b64_tr_b16 a[212:213], v18 offset:0\n"
    "v_max_f32 v21, v64, v65\n"
    "v_mfma_f32_16x16x32_bf16 v[76:79], a[180:183], a[100:103], v[76:79]\n"
    "ds_read_b64_tr_b16 a[214:215], v18 offset:1024\n"
    "v_max3_f32 v21, v21, v66, v67\n"
    "v_mfma_f32_16x16x32_bf16 v[72:75], a[168:171], a[104:107], v[72:75]\n"
    "ds_read_b64_tr_b16 a[216:217], v19 offset:0\n"
    "v_max3_f32 v21, v21, v68, v69\n"
    "v_mfma_f32_16x16x32_bf16 v[76:79], a[184:187], a[104:107], v[76:79]\n"
    "ds_read_b64_tr_b16 a[218:219], v19 offset:1024\n"
    "v_max3_f32 v21, v21, v70, v71\n"
    "v_mfma_f32_16x16x32_bf16 v[72:75], a[172:175], a[108:111], v[72:75]\n"
    "v_exp_f32 v64, v64\n"
    "v_mfma_f32_16x16x32_bf16 v[76:79], a[188:191], a[108:111], v[76:79]\n"
    "v_exp_f32 v65, v65\n"
    "v_mfma_f32_16x16x32_bf16 v[80:83], a[160:163], a[112:115], v[32:35]\n"
    "ds_read_b64_tr_b16 a[220:221], v20 offset:0\n"
    "v_cvt_pk_bf16_f32 v44, v64, v65\n"
    "v_mfma_f32_16x16x32_bf16 v[84:87], a[176:179], a[112:115], v[32:35]\n"
    "v_exp_f32 v66, v66\n"
    "v_mfma_f32_16x16x32_bf16 v[80:83], a[164:167], a[116:119], v[80:83]\n"
    "ds_read_b64_tr_b16 a[222:223], v20 offset:1024\n"
    "v_max3_f32 v21, v21, v72, v73\n"
    "v_mfma_f32_16x16x32_bf16 v[84:87], a[180:183], a[116:119], v[84:87]\n"
    "v_max3_f32 v21, v21, v74, v75\n"
    "v_max3_f32 v21, v21, v76, v77\n"
    "v_mfma_f32_16x16x32_bf16 v[80:83], a[168:171], a[120:123], v[80:83]\n"
    "v_exp_f32 v67, v67\n"
    "v_mfma_f32_16x16x32_bf16 v[84:87], a[184:187], a[120:123], v[84:87]\n"
    "v_max3_f32 v21, v21, v78, v79\n"
    "v_cvt_pk_bf16_f32 v45, v66, v67\n"
    "v_mfma_f32_16x16x32_bf16 v[80:83], a[172:175], a[124:127], v[80:83]\n"
    "v_exp_f32 v68, v68\n"
    "v_mfma_f32_16x16x32_bf16 v[84:87], a[188:191], a[124:127], v[84:87]\n"
    "v_exp_f32 v69, v69\n"
    "v_mfma_f32_16x16x32_bf16 v[88:91], a[160:163], a[128:131], v[36:39]\n"
    "v_exp_f32 v70, v70\n"
    "v_mfma_f32_16x16x32_bf16 v[92:95], a[176:179], a[128:131], v[36:39]\n"
    "v_exp_f32 v71, v71\n"
    "v_mfma_f32_16x16x32_bf16 v[88:91], a[164:167], a[132:135], v[88:91]\n"
    "v_max3_f32 v21, v21, v80, v81\n"
    "v_max3_f32 v21, v21, v82, v83\n"
    "v_mfma_f32_16x16x32_bf16 v[92:95], a[180:183], a[132:135], v[92:95]\n"
    "v_max3_f32 v21, v21, v84, v85\n"
    "v_max3_f32 v21, v21, v86, v87\n"
    "v_mfma_f32_16x16x32_bf16 v[88:91], a[168:171], a[136:139], v[88:91]\n"
    "v_cvt_pk_bf16_f32 v46, v68, v69\n"
    "v_cvt_pk_bf16_f32 v47, v70, v71\n"
    "v_mfma_f32_16x16x32_bf16 v[92:95], a[184:187], a[136:139], v[92:95]\n"
    "v_exp_f32 v72, v72\n"
    "v_mfma_f32_16x16x32_bf16 v[88:91], a[172:175], a[140:143], v[88:91]\n"
    "v_exp_f32 v73, v73\n"
    "v_mfma_f32_16x16x32_bf16 v[92:95], a[188:191], a[140:143], v[92:95]\n"
    "v_exp_f32 v74, v74\n"
    "v_mfma_f32_16x16x32_bf16 v[96:99], a[160:163], a[144:147], v[40:43]\n"
    "v_exp_f32 v75, v75\n"
    "v_mfma_f32_16x16x32_bf16 v[100:103], a[176:179], a[144:147], v[40:43]\n"
    "v_cvt_pk_bf16_f32 v48, v72, v73\n"
    "v_cvt_pk_bf16_f32 v49, v74, v75\n"
    "v_mfma_f32_16x16x32_bf16 v[96:99], a[164:167], a[148:151], v[96:99]\n"
    "v_max3_f32 v21, v21, v88, v89\n"
    "v_max3_f32 v21, v21, v90, v91\n"
    "v_mfma_f32_16x16x32_bf16 v[100:103], a[180:183], a[148:151], v[100:103]\n"
    "v_max3_f32 v21, v21, v92, v93\n"
    "v_max3_f32 v21, v21, v94, v95\n"
    "v_mfma_f32_16x16x32_bf16 v[96:99], a[168:171], a[152:155], v[96:99]\n"
    "v_exp_f32 v76, v76\n"
    "v_mfma_f32_16x16x32_bf16 v[100:103], a[184:187], a[152:155], v[100:103]\n"
    "v_exp_f32 v77, v77\n"
    "v_mfma_f32_16x16x32_bf16 v[96:99], a[172:175], a[156:159], v[96:99]\n"
    "v_cmp_lt_f32 vcc, s63, v21\n"
    "v_cvt_pk_bf16_f32 v50, v76, v77\n"
    "v_mfma_f32_16x16x32_bf16 v[100:103], a[188:191], a[156:159], v[100:103]\n"
    "v_exp_f32 v78, v78\n"
    "v_exp_f32 v79, v79\n"
    "s_nop 0\n"
    "v_cvt_pk_bf16_f32 v51, v78, v79\n"
    "s_cbranch_vccnz LSLOW%=\n"
    "LPHBA%=:\n"
    "s_waitcnt lgkmcnt(0)\n"
    "v_mfma_f32_16x16x32_bf16 v[128:131], a[192:195], v[44:47], v[128:131]\n"
    "s_mov_b32 m0, s71\n"
    "v_exp_f32 v80, v80\n"
    "v_mfma_f32_16x16x32_bf16 v[148:151], a[196:199], v[44:47], v[148:151]\n"
    "buffer_load_dwordx4 v3, s[36:39], s60 offen offset:0 nt lds\n"
    "v_max_f32 v22, v96, v97\n"
    "v_mfma_f32_16x16x32_bf16 v[168:171], a[200:203], v[44:47], v[168:171]\n"
    "buffer_load_dwordx4 v3, s[36:39], s60 offen offset:1024 nt lds\n"
    "v_max3_f32 v22, v22, v98, v99\n"
    "v_mfma_f32_16x16x32_bf16 v[188:191], a[204:207], v[44:47], v[188:191]\n"
    "s_add_u32 s59, s59, 8192\n"
    "v_exp_f32 v81, v81\n"
    "v_mfma_f32_16x16x32_bf16 a[0:3], a[208:211], v[44:47], a[0:3]\n"
    "s_sub_u32 s64, s59, 40960\n"
    "v_exp_f32 v82, v82\n"
    "v_mfma_f32_16x16x32_bf16 a[20:23], a[212:215], v[44:47], a[20:23]\n"
    "s_add_u32 s58, s58, 8192\n"
    "v_exp_f32 v83, v83\n"
    "v_mfma_f32_16x16x32_bf16 a[40:43], a[216:219], v[44:47], a[40:43]\n"
    "buffer_load_dwordx4 v4, s[36:39], s60 offen offset:2048 nt lds\n"
    "v_cvt_pk_bf16_f32 v52, v80, v81\n"
    "v_mfma_f32_16x16x32_bf16 a[60:63], a[220:223], v[44:47], a[60:63]\n"
    "s_add_u32 s70, s70, 1\n"
    "v_cvt_pk_bf16_f32 v53, v82, v83\n"
    "v_max3_f32 v22, v22, v100, v101\n"
    "v_mfma_f32_16x16x32_bf16 a[224:227], v[104:107], v[44:47], a[224:227]\n"
    "s_add_u32 s62, s62, 1\n"
    "v_exp_f32 v84, v84\n"
    "v_mfma_f32_16x16x32_bf16 v[132:135], a[192:195], v[48:51], v[132:135]\n"
    "v_exp_f32 v85, v85\n"
    "v_mfma_f32_16x16x32_bf16 v[152:155], a[196:199], v[48:51], v[152:155]\n"
    "v_exp_f32 v86, v86\n"
    "v_mfma_f32_16x16x32_bf16 v[172:175], a[200:203], v[48:51], v[172:175]\n"
    "buffer_load_dwordx4 v4, s[36:39], s60 offen offset:3072 nt lds\n"
    "v_cvt_pk_bf16_f32 v54, v84, v85\n"
    "v_mfma_f32_16x16x32_bf16 v[192:195], a[204:207], v[48:51], v[192:195]\n"
    "s_add_u32 m0, s71, 4096\n"
    "v_exp_f32 v87, v87\n"
    "v_mfma_f32_16x16x32_bf16 a[4:7], a[208:211], v[48:51], a[4:7]\n"
    "s_add_u32 s71, s71, 8192\n"
    "v_cvt_pk_bf16_f32 v55, v86, v87\n"
    "v_max3_f32 v22, v22, v102, v103\n"
    "v_mfma_f32_16x16x32_bf16 a[24:27], a[212:215], v[48:51], a[24:27]\n"
    "s_add_u32 s60, s69, s70\n"
    "v_exp_f32 v88, v88\n"
    "v_mfma_f32_16x16x32_bf16 a[44:47], a[216:219], v[48:51], a[44:47]\n"
    "buffer_load_dwordx4 v5, s[36:39], s61 offen offset:0 nt lds\n"
    "v_cmp_lt_f32 vcc, s63, v22\n"
    "v_mfma_f32_16x16x32_bf16 a[64:67], a[220:223], v[48:51], a[64:67]\n"
    "s_cbranch_vccnz LS4A%=\n"
    "LS4XA%=:\n"
    "v_exp_f32 v89, v89\n"
    "v_mfma_f32_16x16x32_bf16 a[228:231], v[104:107], v[48:51], a[228:231]\n"
    "s_and_b32 s60, s60, 15\n"
    "v_exp_f32 v90, v90\n"
    "v_mfma_f32_16x16x32_bf16 v[136:139], a[192:195], v[52:55], v[136:139]\n"
    "s_lshl_b32 s60, s60, 13\n"
    "v_exp_f32 v91, v91\n"
    "v_mfma_f32_16x16x32_bf16 v[156:159], a[196:199], v[52:55], v[156:159]\n"
    "buffer_load_dwordx4 v5, s[36:39], s61 offen offset:1024 nt lds\n"
    "v_cvt_pk_bf16_f32 v56, v88, v89\n"
    "v_mfma_f32_16x16x32_bf16 v[176:179], a[200:203], v[52:55], v[176:179]\n"
    "v_exp_f32 v92, v92\n"
    "v_mfma_f32_16x16x32_bf16 v[196:199], a[204:207], v[52:55], v[196:199]\n"
    "s_cmp_ge_u32 s59, s76\n"
    "s_cselect_b32 s59, s64, s59\n"
    "v_cvt_pk_bf16_f32 v57, v90, v91\n"
    "v_mfma_f32_16x16x32_bf16 a[8:11], a[208:211], v[52:55], a[8:11]\n"
    "s_sub_u32 s64, s58, 40960\n"
    "v_exp_f32 v93, v93\n"
    "v_mfma_f32_16x16x32_bf16 a[28:31], a[212:215], v[52:55], a[28:31]\n"
    "v_exp_f32 v94, v94\n"
    "v_mfma_f32_16x16x32_bf16 a[48:51], a[216:219], v[52:55], a[48:51]\n"
    "v_exp_f32 v95, v95\n"
    "v_mfma_f32_16x16x32_bf16 a[68:71], a[220:223], v[52:55], a[68:71]\n"
    "v_cvt_pk_bf16_f32 v58, v92, v93\n"
    "v_cvt_pk_bf16_f32 v59, v94, v95\n"
    "v_mfma_f32_16x16x32_bf16 a[232:235], v[104:107], v[52:55], a[232:235]\n"
    "v_exp_f32 v96, v96\n"
    "v_mfma_f32_16x16x32_bf16 v[140:143], a[192:195], v[56:59], v[140:143]\n"
    "v_exp_f32 v97, v97\n"
    "v_mfma_f32_16x16x32_bf16 v[160:163], a[196:199], v[56:59], v[160:163]\n"
    "buffer_load_dwordx4 v6, s[36:39], s61 offen offset:2048 nt lds\n"
    "v_cvt_pk_bf16_f32 v60, v96, v97\n"
    "v_mfma_f32_16x16x32_bf16 v[180:183], a[200:203], v[56:59], v[180:183]\n"
    "v_exp_f32 v98, v98\n"
    "v_exp_f32 v99, v99\n"
    "v_mfma_f32_16x16x32_bf16 v[200:203], a[204:207], v[56:59], v[200:203]\n"
    "buffer_load_dwordx4 v6, s[36:39], s61 offen offset:3072 nt lds\n"
    "v_cvt_pk_bf16_f32 v61, v98, v99\n"
    "v_mfma_f32_16x16x32_bf16 a[12:15], a[208:211], v[56:59], a[12:15]\n"
    "v_exp_f32 v100, v100\n"
    "v_mfma_f32_16x16x32_bf16 a[32:35], a[212:215], v[56:59], a[32:35]\n"
    "v_exp_f32 v101, v101\n"
    "v_mfma_f32_16x16x32_bf16 a[52:55], a[216:219], v[56:59], a[52:55]\n"
    "ds_read_b128 a[160:163], v9 offset:0\n"
    "v_exp_f32 v102, v102\n"
    "v_exp_f32 v103, v103\n"
    "v_mfma_f32_16x16x32_bf16 a[72:75], a[220:223], v[56:59], a[72:75]\n"
    "ds_read_b128 a[176:179], v9 offset:1024\n"
    "v_cvt_pk_bf16_f32 v62, v100, v101\n"
    "v_cvt_pk_bf16_f32 v63, v102, v103\n"
    "v_mfma_f32_16x16x32_bf16 a[236:239], v[104:107], v[56:59], a[236:239]\n"
    "ds_read_b128 a[164:167], v10 offset:0\n"
    "v_add_u32 v13, s59, v2\n"
    "v_mfma_f32_16x16x32_bf16 v[144:147], a[192:195], v[60:63], v[144:147]\n"
    "ds_read_b128 a[180:183], v10 offset:1024\n"
    "v_xor_b32 v14, 0x20, v13\n"
    "v_mfma_f32_16x16x32_bf16 v[164:167], a[196:199], v[60:63], v[164:167]\n"
    "ds_read_b128 a[168:171], v11 offset:0\n"
    "v_xor_b32 v15, 0x40, v13\n"
    "v_mfma_f32_16x16x32_bf16 v[184:187], a[200:203], v[60:63], v[184:187]\n"
    "ds_read_b128 a[184:187], v11 offset:1024\n"
    "v_xor_b32 v16, 0x60, v13\n"
    "v_mfma_f32_16x16x32_bf16 v[204:207], a[204:207], v[60:63], v[204:207]\n"
    "ds_read_b128 a[172:175], v12 offset:0\n"
    "v_xor_b32 v17, 0x80, v13\n"
    "v_mfma_f32_16x16x32_bf16 a[16:19], a[208:211], v[60:63], a[16:19]\n"
    "s_cmp_ge_u32 s58, s75\n"
    "s_cselect_b32 s58, s64, s58\n"
    "ds_read_b128 a[188:191], v12 offset:1024\n"
    "v_mfma_f32_16x16x32_bf16 a[36:39], a[212:215], v[60:63], a[36:39]\n"
    "s_sub_u32 s64, s71, 40960\n"
    "v_xor_b32 v18, 0xa0, v13\n"
    "v_xor_b32 v19, 0xc0, v13\n"
    "v_mfma_f32_16x16x32_bf16 a[56:59], a[216:219], v[60:63], a[56:59]\n"
    "s_cmp_ge_u32 s71, s77\n"
    "s_cselect_b32 s71, s64, s71\n"
    "s_cmp_lt_u32 s70, 16\n"
    "s_cselect_b32 s60, s60, 0x7ff00000\n"
    "s_add_u32 s61, s60, 4096\n"
    "v_mfma_f32_16x16x32_bf16 a[76:79], a[220:223], v[60:63], a[76:79]\n"
    "s_cmp_ge_u32 s70, 16\n"
    "s_cbranch_scc1 LSPA%=\n"
    "LSPAX%=:\n"
    "v_xor_b32 v20, 0xe0, v13\n"
    "v_mfma_f32_16x16x32_bf16 a[240:243], v[104:107], v[60:63], a[240:243]\n"
    "s_cmp_lt_u32 s62, 16\n"
    "s_waitcnt vmcnt(24) lgkmcnt(0)\n"
    "s_barrier\n"
    "s_cbranch_scc1 LLOOPA%=\n"
    "s_branch LEPIA%=\n"
    "LSPEC%=:\n"
    "s_cmp_gt_u32 s70, 3\n"
    "s_cbranch_scc1 LSPEC1%=\n"
    "s_cmp_eq_u32 s70, 2\n"
    "s_cselect_b32 s38, 0, 131072\n"
    "s_cselect_b32 s71, 0x3e000, s71\n"
    "s_branch LSPECX%=\n"
    "LSPEC1%=:\n"
    "s_cmp_eq_u32 s70, 16\n"
    "s_cbranch_scc0 LSPEC2%=\n"
    "s_add_u32 s66, %2, 0\n"
    "s_and_b32 s66, s66, 3\n"
    "s_lshl_b32 s66, s66, 16\n"
    "s_lshr_b32 s67, s20, 2\n"
    "s_xor_b32 s67, s67, 0\n"
    "s_lshl_b32 s67, s67, 7\n"
    "s_add_u32 s66, s66, s67\n"
    "s_add_u32 s36, s12, s66\n"
    "s_addc_u32 s37, s13, 0\n"
    "s_and_b32 s37, s37, 0xffff\n"
    "s_mov_b32 s38, 64640\n"
    "s_mov_b32 s39, 0x20000\n"
    "s_add_u32 s66, %2, 1\n"
    "s_and_b32 s66, s66, 3\n"
    "s_lshl_b32 s66, s66, 16\n"
    "s_lshr_b32 s67, s20, 2\n"
    "s_xor_b32 s67, s67, 0\n"
    "s_lshl_b32 s67, s67, 7\n"
    "s_add_u32 s66, s66, s67\n"
    "s_add_u32 s40, s12, s66\n"
    "s_addc_u32 s41, s13, 0\n"
    "s_and_b32 s41, s41, 0xffff\n"
    "s_mov_b32 s42, 64640\n"
    "s_mov_b32 s43, 0x20000\n"
    "v_mov_b32 v3, v222\n"
    "v_mov_b32 v4, v223\n"
    "v_mov_b32 v5, v222\n"
    "v_mov_b32 v6, v223\n"
    "v_mov_b32 v7, v222\n"
    "v_mov_b32 v8, v223\n"
    "s_mov_b32 s60, 0\n"
    "s_mov_b32 s61, 32768\n"
    "s_add_u32 s71, s56, 24576\n"
    "s_add_u32 s24, s56, 16384\n"
    "s_mov_b32 s44, s12\n"
    "s_and_b32 s45, s13, 0xffff\n"
    "s_mov_b32 s46, 261376\n"
    "s_mov_b32 s47, 0x20000\n"
    "v_lshlrev_b32 v123, 3, v0\n"
    "v_add_u32 v116, s56, v123\n"
    "v_add_u32 v117, s57, v123\n"
    "v_lshlrev_b32 v123, 4, v0\n"
    "s_lshl_b32 s69, s20, 1\n"
    "s_add_u32 s69, s69, s34\n"
    "s_lshl_b32 s69, s69, 4\n"
    "s_add_u32 s69, s69, s21\n"
    "s_mul_i32 s66, s69, 81920\n"
    "s_mul_i32 s67, %2, 20480\n"
    "s_add_u32 s66, s66, s67\n"
    "s_add_u32 s52, s14, s66\n"
    "s_addc_u32 s53, s15, 0\n"
    "s_and_b32 s53, s53, 0xffff\n"
    "s_mov_b32 s54, 20480\n"
    "s_mov_b32 s55, 0x20000\n"
    "s_add_u32 s66, %2, 0\n"
    "s_and_b32 s66, s66, 3\n"
    "s_xor_b32 s66, s66, 1\n"
    "s_mul_i32 s66, s66, 40960\n"
    "v_add_u32 v118, s66, v123\n"
    "s_add_u32 s66, %2, 1\n"
    "s_and_b32 s66, s66, 3\n"
    "s_xor_b32 s66, s66, 1\n"
    "s_mul_i32 s66, s66, 40960\n"
    "v_add_u32 v119, s66, v123\n"
    "s_add_u32 s66, %2, 2\n"
    "s_and_b32 s66, s66, 3\n"
    "s_xor_b32 s66, s66, 1\n"
    "s_mul_i32 s66, s66, 40960\n"
    "v_add_u32 v120, s66, v123\n"
    "s_add_u32 s66, %2, 3\n"
    "s_and_b32 s66, s66, 3\n"
    "s_xor_b32 s66, s66, 1\n"
    "s_mul_i32 s66, s66, 40960\n"
    "v_add_u32 v121, s66, v123\n"
    "s_add_u32 s66, %2, 2\n"
    "s_and_b32 s66, s66, 3\n"
    "s_lshl_b32 s66, s66, 16\n"
    "s_lshr_b32 s67, s20, 2\n"
    "s_xor_b32 s67, s67, 0\n"
    "s_lshl_b32 s67, s67, 7\n"
    "s_add_u32 s66, s66, s67\n"
    "s_add_u32 s67, s66, 32768\n"
    "s_branch LSPECX%=\n"
    "LSPEC2%=:\n"
    "s_cmp_eq_u32 s70, 17\n"
    "s_cbranch_scc0 LSPECX%=\n"
    "s_branch LLAST%=\n"
    "LSPA%=:\n"
    "s_cmp_eq_u32 s70, 16\n"
    "s_cbranch_scc0 LSPA16%=\n"
    "s_lshl_b32 s66, s20, 15\n"
    "s_mul_i32 s67, %2, 7936\n"
    "s_add_u32 s66, s66, s67\n"
    "s_add_u32 s48, s12, s66\n"
    "s_addc_u32 s49, s13, 0\n"
    "s_and_b32 s49, s49, 0xffff\n"
    "s_mov_b32 s50, 8192\n"
    "s_mov_b32 s51, 0x20000\n"
    "v_lshlrev_b32 v122, 4, v0\n"
    "s_movk_i32 s67, 0x1000\n"
    "buffer_load_dwordx4 a[252:255], v122, s[48:51], 0 offen offset:0\n"
    "buffer_load_dwordx4 a[252:255], v122, s[48:51], 0 offen offset:1024\n"
    "buffer_load_dwordx4 a[252:255], v122, s[48:51], 0 offen offset:2048\n"
    "buffer_load_dwordx4 a[252:255], v122, s[48:51], 0 offen offset:3072\n"
    "buffer_load_dwordx4 a[252:255], v122, s[48:51], s67 offen offset:0\n"
    "buffer_load_dwordx4 a[252:255], v122, s[48:51], s67 offen offset:1024\n"
    "buffer_load_dwordx4 a[252:255], v122, s[48:51], s67 offen offset:2048\n"
    "buffer_load_dwordx4 a[252:255], v122, s[48:51], s67 offen offset:3072\n"
    "s_branch LSPAX%=\n"
    "LSPA16%=:\n"
    "s_cmp_eq_u32 s70, 17\n"
    "s_cbranch_scc0 LSPA17%=\n"
    "s_add_u32 s66, %2, 1\n"
    "s_and_b32 s66, s66, 3\n"
    "s_lshl_b32 s66, s66, 16\n"
    "s_lshr_b32 s67, s20, 2\n"
    "s_xor_b32 s67, s67, 0\n"
    "s_lshl_b32 s67, s67, 7\n"
    "s_add_u32 s66, s66, s67\n"
    "s_add_u32 s36, s12, s66\n"
    "s_addc_u32 s37, s13, 0\n"
    "s_and_b32 s37, s37, 0xffff\n"
    "s_mov_b32 s38, 64640\n"
    "s_mov_b32 s39, 0x20000\n"
    "v_mov_b32 v3, v222\n"
    "v_mov_b32 v4, v223\n"
    "v_mov_b32 v5, v222\n"
    "v_mov_b32 v6, v223\n"
    "s_mov_b32 s60, 0\n"
    "s_mov_b32 s61, 32768\n"
    "s_branch LSPAX%=\n"
    "LSPA17%=:\n"
    "s_cmp_eq_u32 s70, 18\n"
    "s_cbranch_scc0 LSPA18%=\n"
    "s_add_u32 s66, %2, 0\n"
    "s_and_b32 s66, s66, 3\n"
    "s_lshl_b32 s66, s66, 16\n"
    "s_lshr_b32 s67, s20, 2\n"
    "s_xor_b32 s67, s67, 0\n"
    "s_lshl_b32 s67, s67, 7\n"
    "s_add_u32 s66, s66, s67\n"
    "s_add_u32 s36, s12, s66\n"
    "s_addc_u32 s37, s13, 0\n"
    "s_and_b32 s37, s37, 0xffff\n"
    "s_mov_b32 s38, 64640\n"
    "s_mov_b32 s39, 0x20000\n"
    "s_mov_b32 s60, 0\n"
    "s_mov_b32 s61, 32768\n"
    "s_branch LSPAX%=\n"
    "LSPA18%=:\n"
    "s_cmp_eq_u32 s70, 19\n"
    "s_cbranch_scc0 LSPA19%=\n"
    "s_add_u32 s66, %2, 2\n"
    "s_and_b32 s66, s66, 3\n"
    "s_lshl_b32 s66, s66, 16\n"
    "s_lshr_b32 s67, s20, 2\n"
    "s_xor_b32 s67, s67, 0\n"
    "s_lshl_b32 s67, s67, 7\n"
    "s_add_u32 s66, s66, s67\n"
    "s_add_u32 s36, s12, s66\n"
    "s_addc_u32 s37, s13, 0\n"
    "s_and_b32 s37, s37, 0xffff\n"
    "s_mov_b32 s38, 64640\n"
    "s_mov_b32 s39, 0x20000\n"
    "s_mov_b32 s60, 0\n"
    "s_mov_b32 s61, 32768\n"
    "s_branch LSPAX%=\n"
    "LSPA19%=:\n"
    "s_cmp_ge_u32 s70, 20\n"
    "s_cselect_b32 s71, 0x3e000, s71\n"
    "s_branch LSPAX%=\n"
    "LSLOW%=:\n"
    "s_nop 11\n"
    "s_waitcnt lgkmcnt(0)\n"
    "v_mfma_f32_16x16x32_bf16 v[64:67], a[160:163], a[80:83], v[24:27]\n"
    "v_mfma_f32_16x16x32_bf16 v[68:71], a[176:179], a[80:83], v[24:27]\n"
    "v_mfma_f32_16x16x32_bf16 v[64:67], a[164:167], a[84:87], v[64:67]\n"
    "v_mfma_f32_16x16x32_bf16 v[68:71], a[180:183], a[84:87], v[68:71]\n"
    "v_mfma_f32_16x16x32_bf16 v[64:67], a[168:171], a[88:91], v[64:67]\n"
    "v_mfma_f32_16x16x32_bf16 v[68:71], a[184:187], a[88:91], v[68:71]\n"
    "v_mfma_f32_16x16x32_bf16 v[64:67], a[172:175], a[92:95], v[64:67]\n"
    "v_mfma_f32_16x16x32_bf16 v[68:71], a[188:191], a[92:95], v[68:71]\n"
    "v_mfma_f32_16x16x32_bf16 v[72:75], a[160:163], a[96:99], v[28:31]\n"
    "v_mfma_f32_16x16x32_bf16 v[76:79], a[176:179], a[96:99], v[28:31]\n"
    "v_mfma_f32_16x16x32_bf16 v[72:75], a[164:167], a[100:103], v[72:75]\n"
    "v_mfma_f32_16x16x32_bf16 v[76:79], a[180:183], a[100:103], v[76:79]\n"
    "v_mfma_f32_16x16x32_bf16 v[72:75], a[168:171], a[104:107], v[72:75]\n"
    "v_mfma_f32_16x16x32_bf16 v[76:79], a[184:187], a[104:107], v[76:79]\n"
    "v_mfma_f32_16x16x32_bf16 v[72:75], a[172:175], a[108:111], v[72:75]\n"
    "v_max3_f32 v244, v64, v65, v66\n"
    "v_max3_f32 v245, v68, v69, v70\n"
    "v_max3_f32 v244, v244, v67, v71\n"
    "v_max_f32 v244, v244, v245\n"
    "v_mfma_f32_16x16x32_bf16 v[76:79], a[188:191], a[108:111], v[76:79]\n"
    "v_mov_b32 v245, v244\n"
    "v_mfma_f32_16x16x32_bf16 v[80:83], a[160:163], a[112:115], v[32:35]\n"
    "v_mfma_f32_16x16x32_bf16 v[84:87], a[176:179], a[112:115], v[32:35]\n"
    "v_permlane32_swap_b32 v244, v245\n"
    "v_max_f32 v244, v244, v245\n"
    "v_mov_b32 v245, v244\n"
    "v_max3_f32 v246, v72, v73, v74\n"
    "v_mfma_f32_16x16x32_bf16 v[80:83], a[164:167], a[116:119], v[80:83]\n"
    "v_permlane16_swap_b32 v244, v245\n"
    "v_max3_f32 v244, v244, v245, s74\n"
    "v_sub_f32 v64, v64, v244\n"
    "v_sub_f32 v65, v65, v244\n"
    "v_mfma_f32_16x16x32_bf16 v[84:87], a[180:183], a[116:119], v[84:87]\n"
    "v_sub_f32 v66, v66, v244\n"
    "v_sub_f32 v67, v67, v244\n"
    "v_sub_f32 v68, v68, v244\n"
    "v_sub_f32 v69, v69, v244\n"
    "v_mfma_f32_16x16x32_bf16 v[80:83], a[168:171], a[120:123], v[80:83]\n"
    "v_sub_f32 v70, v70, v244\n"
    "v_sub_f32 v71, v71, v244\n"
    "v_sub_f32 v24, v24, v244\n"
    "v_sub_f32 v25, v25, v244\n"
    "v_mfma_f32_16x16x32_bf16 v[84:87], a[184:187], a[120:123], v[84:87]\n"
    "v_sub_f32 v26, v26, v244\n"
    "v_sub_f32 v27, v27, v244\n"
    "v_max_f32 v245, 0, v244\n"
    "v_max3_f32 v247, v76, v77, v78\n"
    "v_mfma_f32_16x16x32_bf16 v[80:83], a[172:175], a[124:127], v[80:83]\n"
    "v_max3_f32 v246, v246, v75, v79\n"
    "v_max_f32 v246, v246, v247\n"
    "v_mov_b32 v247, v246\n"
    "v_mfma_f32_16x16x32_bf16 v[84:87], a[188:191], a[124:127], v[84:87]\n"
    "v_exp_f32 v216, -v245\n"
    "v_permlane32_swap_b32 v246, v247\n"
    "v_max_f32 v246, v246, v247\n"
    "v_mfma_f32_16x16x32_bf16 v[88:91], a[160:163], a[128:131], v[36:39]\n"
    "v_mov_b32 v247, v246\n"
    "v_max3_f32 v248, v80, v81, v82\n"
    "v_mfma_f32_16x16x32_bf16 v[92:95], a[176:179], a[128:131], v[36:39]\n"
    "v_permlane16_swap_b32 v246, v247\n"
    "v_max3_f32 v246, v246, v247, s74\n"
    "v_sub_f32 v72, v72, v246\n"
    "v_sub_f32 v73, v73, v246\n"
    "v_mfma_f32_16x16x32_bf16 v[88:91], a[164:167], a[132:135], v[88:91]\n"
    "v_sub_f32 v74, v74, v246\n"
    "v_sub_f32 v75, v75, v246\n"
    "v_sub_f32 v76, v76, v246\n"
    "v_sub_f32 v77, v77, v246\n"
    "v_mfma_f32_16x16x32_bf16 v[92:95], a[180:183], a[132:135], v[92:95]\n"
    "v_sub_f32 v78, v78, v246\n"
    "v_sub_f32 v79, v79, v246\n"
    "v_sub_f32 v28, v28, v246\n"
    "v_sub_f32 v29, v29, v246\n"
    "v_mfma_f32_16x16x32_bf16 v[88:91], a[168:171], a[136:139], v[88:91]\n"
    "v_sub_f32 v30, v30, v246\n"
    "v_sub_f32 v31, v31, v246\n"
    "v_max_f32 v247, 0, v246\n"
    "v_max3_f32 v249, v84, v85, v86\n"
    "v_mfma_f32_16x16x32_bf16 v[92:95], a[184:187], a[136:139], v[92:95]\n"
    "v_max3_f32 v248, v248, v83, v87\n"
    "v_max_f32 v248, v248, v249\n"
    "v_mov_b32 v249, v248\n"
    "v_mfma_f32_16x16x32_bf16 v[88:91], a[172:175], a[140:143], v[88:91]\n"
    "v_exp_f32 v64, v64\n"
    "v_permlane32_swap_b32 v248, v249\n"
    "v_max_f32 v248, v248, v249\n"
    "v_mfma_f32_16x16x32_bf16 v[92:95], a[188:191], a[140:143], v[92:95]\n"
    "v_exp_f32 v65, v65\n"
    "v_mov_b32 v249, v248\n"
    "v_cvt_pk_bf16_f32 v44, v64, v65\n"
    "v_mfma_f32_16x16x32_bf16 v[96:99], a[160:163], a[144:147], v[40:43]\n"
    "v_max3_f32 v250, v88, v89, v90\n"
    "v_permlane16_swap_b32 v248, v249\n"
    "v_max3_f32 v248, v248, v249, s74\n"
    "v_sub_f32 v80, v80, v248\n"
    "v_mfma_f32_16x16x32_bf16 v[100:103], a[176:179], a[144:147], v[40:43]\n"
    "v_max3_f32 v251, v92, v93, v94\n"
    "v_max3_f32 v250, v250, v91, v95\n"
    "v_max_f32 v250, v250, v251\n"
    "v_mov_b32 v251, v250\n"
    "v_mfma_f32_16x16x32_bf16 v[96:99], a[164:167], a[148:151], v[96:99]\n"
    "v_sub_f32 v81, v81, v248\n"
    "v_permlane32_swap_b32 v250, v251\n"
    "v_max_f32 v250, v250, v251\n"
    "v_mov_b32 v251, v250\n"
    "v_mfma_f32_16x16x32_bf16 v[100:103], a[180:183], a[148:151], v[100:103]\n"
    "v_sub_f32 v82, v82, v248\n"
    "v_permlane16_swap_b32 v250, v251\n"
    "v_max3_f32 v250, v250, v251, s74\n"
    "v_sub_f32 v88, v88, v250\n"
    "v_mfma_f32_16x16x32_bf16 v[96:99], a[168:171], a[152:155], v[96:99]\n"
    "v_sub_f32 v89, v89, v250\n"
    "v_sub_f32 v90, v90, v250\n"
    "v_sub_f32 v83, v83, v248\n"
    "v_sub_f32 v91, v91, v250\n"
    "v_mfma_f32_16x16x32_bf16 v[100:103], a[184:187], a[152:155], v[100:103]\n"
    "v_sub_f32 v84, v84, v248\n"
    "v_sub_f32 v92, v92, v250\n"
    "v_sub_f32 v85, v85, v248\n"
    "v_sub_f32 v93, v93, v250\n"
    "v_mfma_f32_16x16x32_bf16 v[96:99], a[172:175], a[156:159], v[96:99]\n"
    "v_sub_f32 v86, v86, v248\n"
    "v_sub_f32 v94, v94, v250\n"
    "v_sub_f32 v87, v87, v248\n"
    "v_sub_f32 v95, v95, v250\n"
    "v_mfma_f32_16x16x32_bf16 v[100:103], a[188:191], a[156:159], v[100:103]\n"
    "v_sub_f32 v32, v32, v248\n"
    "v_sub_f32 v36, v36, v250\n"
    "v_sub_f32 v33, v33, v248\n"
    "v_max3_f32 v252, v96, v97, v98\n"
    "v_exp_f32 v66, v66\n"
    "v_exp_f32 v67, v67\n"
    "s_nop 0\n"
    "v_cvt_pk_bf16_f32 v45, v66, v67\n"
    "v_exp_f32 v68, v68\n"
    "v_exp_f32 v69, v69\n"
    "v_exp_f32 v70, v70\n"
    "v_exp_f32 v71, v71\n"
    "v_cvt_pk_bf16_f32 v46, v68, v69\n"
    "v_cvt_pk_bf16_f32 v47, v70, v71\n"
    "v_exp_f32 v217, -v247\n"
    "v_exp_f32 v72, v72\n"
    "v_exp_f32 v73, v73\n"
    "v_exp_f32 v74, v74\n"
    "v_exp_f32 v75, v75\n"
    "v_cvt_pk_bf16_f32 v48, v72, v73\n"
    "v_cvt_pk_bf16_f32 v49, v74, v75\n"
    "v_exp_f32 v76, v76\n"
    "v_exp_f32 v77, v77\n"
    "v_exp_f32 v78, v78\n"
    "v_exp_f32 v79, v79\n"
    "v_cvt_pk_bf16_f32 v50, v76, v77\n"
    "v_cvt_pk_bf16_f32 v51, v78, v79\n"
    "v_max3_f32 v253, v100, v101, v102\n"
    "v_max3_f32 v252, v252, v99, v103\n"
    "v_max_f32 v252, v252, v253\n"
    "v_mov_b32 v253, v252\n"
    "s_nop 1\n"
    "v_permlane32_swap_b32 v252, v253\n"
    "v_max_f32 v252, v252, v253\n"
    "v_mov_b32 v253, v252\n"
    "s_nop 1\n"
    "v_permlane16_swap_b32 v252, v253\n"
    "v_max3_f32 v252, v252, v253, s74\n"
    "v_sub_f32 v96, v96, v252\n"
    "v_sub_f32 v97, v97, v252\n"
    "v_sub_f32 v98, v98, v252\n"
    "v_sub_f32 v99, v99, v252\n"
    "v_sub_f32 v100, v100, v252\n"
    "v_sub_f32 v101, v101, v252\n"
    "v_sub_f32 v102, v102, v252\n"
    "v_sub_f32 v103, v103, v252\n"
    "v_sub_f32 v40, v40, v252\n"
    "v_sub_f32 v37, v37, v250\n"
    "v_sub_f32 v41, v41, v252\n"
    "v_sub_f32 v34, v34, v248\n"
    "v_sub_f32 v38, v38, v250\n"
    "v_sub_f32 v42, v42, v252\n"
    "v_sub_f32 v35, v35, v248\n"
    "v_sub_f32 v39, v39, v250\n"
    "v_sub_f32 v43, v43, v252\n"
    "v_max_f32 v249, 0, v248\n"
    "v_max_f32 v251, 0, v250\n"
    "v_max_f32 v253, 0, v252\n"
    "v_exp_f32 v218, -v249\n"
    "v_exp_f32 v219, -v251\n"
    "v_exp_f32 v220, -v253\n"
    "s_mov_b32 s74, 0\n"
    "s_cmp_eq_u32 s62, 0\n"
    "s_cbranch_scc1 LSLOWX%=\n"
    "v_mul_f32 v128, v216, v128\n"
    "v_mul_f32 v129, v216, v129\n"
    "v_mul_f32 v130, v216, v130\n"
    "v_mul_f32 v131, v216, v131\n"
    "v_mul_f32 v148, v216, v148\n"
    "v_mul_f32 v149, v216, v149\n"
    "v_mul_f32 v150, v216, v150\n"
    "v_mul_f32 v151, v216, v151\n"
    "v_mul_f32 v168, v216, v168\n"
    "v_mul_f32 v169, v216, v169\n"
    "v_mul_f32 v170, v216, v170\n"
    "v_mul_f32 v171, v216, v171\n"
    "v_mul_f32 v188, v216, v188\n"
    "v_mul_f32 v189, v216, v189\n"
    "v_mul_f32 v190, v216, v190\n"
    "v_mul_f32 v191, v216, v191\n"
    "v_accvgpr_read_b32 v22, a0\n"
    "v_mul_f32 v22, v216, v22\n"
    "v_accvgpr_write_b32 a0, v22\n"
    "v_accvgpr_read_b32 v23, a1\n"
    "v_mul_f32 v23, v216, v23\n"
    "v_accvgpr_write_b32 a1, v23\n"
    "v_accvgpr_read_b32 v22, a2\n"
    "v_mul_f32 v22, v216, v22\n"
    "v_accvgpr_write_b32 a2, v22\n"
    "v_accvgpr_read_b32 v23, a3\n"
    "v_mul_f32 v23, v216, v23\n"
    "v_accvgpr_write_b32 a3, v23\n"
    "v_accvgpr_read_b32 v22, a20\n"
    "v_mul_f32 v22, v216, v22\n"
    "v_accvgpr_write_b32 a20, v22\n"
    "v_accvgpr_read_b32 v23, a21\n"
    "v_mul_f32 v23, v216, v23\n"
    "v_accvgpr_write_b32 a21, v23\n"
    "v_accvgpr_read_b32 v22, a22\n"
    "v_mul_f32 v22, v216, v22\n"
    "v_accvgpr_write_b32 a22, v22\n"
    "v_accvgpr_read_b32 v23, a23\n"
    "v_mul_f32 v23, v216, v23\n"
    "v_accvgpr_write_b32 a23, v23\n"
    "v_accvgpr_read_b32 v22, a40\n"
    "v_mul_f32 v22, v216, v22\n"
    "v_accvgpr_write_b32 a40, v22\n"
    "v_accvgpr_read_b32 v23, a41\n"
    "v_mul_f32 v23, v216, v23\n"
    "v_accvgpr_write_b32 a41, v23\n"
    "v_accvgpr_read_b32 v22, a42\n"
    "v_mul_f32 v22, v216, v22\n"
    "v_accvgpr_write_b32 a42, v22\n"
    "v_accvgpr_read_b32 v23, a43\n"
    "v_mul_f32 v23, v216, v23\n"
    "v_accvgpr_write_b32 a43, v23\n"
    "v_accvgpr_read_b32 v22, a60\n"
    "v_mul_f32 v22, v216, v22\n"
    "v_accvgpr_write_b32 a60, v22\n"
    "v_accvgpr_read_b32 v23, a61\n"
    "v_mul_f32 v23, v216, v23\n"
    "v_accvgpr_write_b32 a61, v23\n"
    "v_accvgpr_read_b32 v22, a62\n"
    "v_mul_f32 v22, v216, v22\n"
    "v_accvgpr_write_b32 a62, v22\n"
    "v_accvgpr_read_b32 v23, a63\n"
    "v_mul_f32 v23, v216, v23\n"
    "v_accvgpr_write_b32 a63, v23\n"
    "v_accvgpr_read_b32 v22, a224\n"
    "v_mul_f32 v22, v216, v22\n"
    "v_accvgpr_write_b32 a224, v22\n"
    "v_accvgpr_read_b32 v23, a225\n"
    "v_mul_f32 v23, v216, v23\n"
    "v_accvgpr_write_b32 a225, v23\n"
    "v_accvgpr_read_b32 v22, a226\n"
    "v_mul_f32 v22, v216, v22\n"
    "v_accvgpr_write_b32 a226, v22\n"
    "v_accvgpr_read_b32 v23, a227\n"
    "v_mul_f32 v23, v216, v23\n"
    "v_accvgpr_write_b32 a227, v23\n"
    "v_mul_f32 v132, v217, v132\n"
    "v_mul_f32 v133, v217, v133\n"
    "v_mul_f32 v134, v217, v134\n"
    "v_mul_f32 v135, v217, v135\n"
    "v_mul_f32 v152, v217, v152\n"
    "v_mul_f32 v153, v217, v153\n"
    "v_mul_f32 v154, v217, v154\n"
    "v_mul_f32 v155, v217, v155\n"
    "v_mul_f32 v172, v217, v172\n"
    "v_mul_f32 v173, v217, v173\n"
    "v_mul_f32 v174, v217, v174\n"
    "v_mul_f32 v175, v217, v175\n"
    "v_mul_f32 v192, v217, v192\n"
    "v_mul_f32 v193, v217, v193\n"
    "v_mul_f32 v194, v217, v194\n"
    "v_mul_f32 v195, v217, v195\n"
    "v_accvgpr_read_b32 v22, a4\n"
    "v_mul_f32 v22, v217, v22\n"
    "v_accvgpr_write_b32 a4, v22\n"
    "v_accvgpr_read_b32 v23, a5\n"
    "v_mul_f32 v23, v217, v23\n"
    "v_accvgpr_write_b32 a5, v23\n"
    "v_accvgpr_read_b32 v22, a6\n"
    "v_mul_f32 v22, v217, v22\n"
    "v_accvgpr_write_b32 a6, v22\n"
    "v_accvgpr_read_b32 v23, a7\n"
    "v_mul_f32 v23, v217, v23\n"
    "v_accvgpr_write_b32 a7, v23\n"
    "v_accvgpr_read_b32 v22, a24\n"
    "v_mul_f32 v22, v217, v22\n"
    "v_accvgpr_write_b32 a24, v22\n"
    "v_accvgpr_read_b32 v23, a25\n"
    "v_mul_f32 v23, v217, v23\n"
    "v_accvgpr_write_b32 a25, v23\n"
    "v_accvgpr_read_b32 v22, a26\n"
    "v_mul_f32 v22, v217, v22\n"
    "v_accvgpr_write_b32 a26, v22\n"
    "v_accvgpr_read_b32 v23, a27\n"
    "v_mul_f32 v23, v217, v23\n"
    "v_accvgpr_write_b32 a27, v23\n"
    "v_accvgpr_read_b32 v22, a44\n"
    "v_mul_f32 v22, v217, v22\n"
    "v_accvgpr_write_b32 a44, v22\n"
    "v_accvgpr_read_b32 v23, a45\n"
    "v_mul_f32 v23, v217, v23\n"
    "v_accvgpr_write_b32 a45, v23\n"
    "v_accvgpr_read_b32 v22, a46\n"
    "v_mul_f32 v22, v217, v22\n"
    "v_accvgpr_write_b32 a46, v22\n"
    "v_accvgpr_read_b32 v23, a47\n"
    "v_mul_f32 v23, v217, v23\n"
    "v_accvgpr_write_b32 a47, v23\n"
    "v_accvgpr_read_b32 v22, a64\n"
    "v_mul_f32 v22, v217, v22\n"
    "v_accvgpr_write_b32 a64, v22\n"
    "v_accvgpr_read_b32 v23, a65\n"
    "v_mul_f32 v23, v217, v23\n"
    "v_accvgpr_write_b32 a65, v23\n"
    "v_accvgpr_read_b32 v22, a66\n"
    "v_mul_f32 v22, v217, v22\n"
    "v_accvgpr_write_b32 a66, v22\n"
    "v_accvgpr_read_b32 v23, a67\n"
    "v_mul_f32 v23, v217, v23\n"
    "v_accvgpr_write_b32 a67, v23\n"
    "v_accvgpr_read_b32 v22, a228\n"
    "v_mul_f32 v22, v217, v22\n"
    "v_accvgpr_write_b32 a228, v22\n"
    "v_accvgpr_read_b32 v23, a229\n"
    "v_mul_f32 v23, v217, v23\n"
    "v_accvgpr_write_b32 a229, v23\n"
    "v_accvgpr_read_b32 v22, a230\n"
    "v_mul_f32 v22, v217, v22\n"
    "v_accvgpr_write_b32 a230, v22\n"
    "v_accvgpr_read_b32 v23, a231\n"
    "v_mul_f32 v23, v217, v23\n"
    "v_accvgpr_write_b32 a231, v23\n"
    "v_mul_f32 v136, v218, v136\n"
    "v_mul_f32 v137, v218, v137\n"
    "v_mul_f32 v138, v218, v138\n"
    "v_mul_f32 v139, v218, v139\n"
    "v_mul_f32 v156, v218, v156\n"
    "v_mul_f32 v157, v218, v157\n"
    "v_mul_f32 v158, v218, v158\n"
    "v_mul_f32 v159, v218, v159\n"
    "v_mul_f32 v176, v218, v176\n"
    "v_mul_f32 v177, v218, v177\n"
    "v_mul_f32 v178, v218, v178\n"
    "v_mul_f32 v179, v218, v179\n"
    "v_mul_f32 v196, v218, v196\n"
    "v_mul_f32 v197, v218, v197\n"
    "v_mul_f32 v198, v218, v198\n"
    "v_mul_f32 v199, v218, v199\n"
    "v_accvgpr_read_b32 v22, a8\n"
    "v_mul_f32 v22, v218, v22\n"
    "v_accvgpr_write_b32 a8, v22\n"
    "v_accvgpr_read_b32 v23, a9\n"
    "v_mul_f32 v23, v218, v23\n"
    "v_accvgpr_write_b32 a9, v23\n"
    "v_accvgpr_read_b32 v22, a10\n"
    "v_mul_f32 v22, v218, v22\n"
    "v_accvgpr_write_b32 a10, v22\n"
    "v_accvgpr_read_b32 v23, a11\n"
    "v_mul_f32 v23, v218, v23\n"
    "v_accvgpr_write_b32 a11, v23\n"
    "v_accvgpr_read_b32 v22, a28\n"
    "v_mul_f32 v22, v218, v22\n"
    "v_accvgpr_write_b32 a28, v22\n"
    "v_accvgpr_read_b32 v23, a29\n"
    "v_mul_f32 v23, v218, v23\n"
    "v_accvgpr_write_b32 a29, v23\n"
    "v_accvgpr_read_b32 v22, a30\n"
    "v_mul_f32 v22, v218, v22\n"
    "v_accvgpr_write_b32 a30, v22\n"
    "v_accvgpr_read_b32 v23, a31\n"
    "v_mul_f32 v23, v218, v23\n"
    "v_accvgpr_write_b32 a31, v23\n"
    "v_accvgpr_read_b32 v22, a48\n"
    "v_mul_f32 v22, v218, v22\n"
    "v_accvgpr_write_b32 a48, v22\n"
    "v_accvgpr_read_b32 v23, a49\n"
    "v_mul_f32 v23, v218, v23\n"
    "v_accvgpr_write_b32 a49, v23\n"
    "v_accvgpr_read_b32 v22, a50\n"
    "v_mul_f32 v22, v218, v22\n"
    "v_accvgpr_write_b32 a50, v22\n"
    "v_accvgpr_read_b32 v23, a51\n"
    "v_mul_f32 v23, v218, v23\n"
    "v_accvgpr_write_b32 a51, v23\n"
    "v_accvgpr_read_b32 v22, a68\n"
    "v_mul_f32 v22, v218, v22\n"
    "v_accvgpr_write_b32 a68, v22\n"
    "v_accvgpr_read_b32 v23, a69\n"
    "v_mul_f32 v23, v218, v23\n"
    "v_accvgpr_write_b32 a69, v23\n"
    "v_accvgpr_read_b32 v22, a70\n"
    "v_mul_f32 v22, v218, v22\n"
    "v_accvgpr_write_b32 a70, v22\n"
    "v_accvgpr_read_b32 v23, a71\n"
    "v_mul_f32 v23, v218, v23\n"
    "v_accvgpr_write_b32 a71, v23\n"
    "v_accvgpr_read_b32 v22, a232\n"
    "v_mul_f32 v22, v218, v22\n"
    "v_accvgpr_write_b32 a232, v22\n"
    "v_accvgpr_read_b32 v23, a233\n"
    "v_mul_f32 v23, v218, v23\n"
    "v_accvgpr_write_b32 a233, v23\n"
    "v_accvgpr_read_b32 v22, a234\n"
    "v_mul_f32 v22, v218, v22\n"
    "v_accvgpr_write_b32 a234, v22\n"
    "v_accvgpr_read_b32 v23, a235\n"
    "v_mul_f32 v23, v218, v23\n"
    "v_accvgpr_write_b32 a235, v23\n"
    "v_mul_f32 v140, v219, v140\n"
    "v_mul_f32 v141, v219, v141\n"
    "v_mul_f32 v142, v219, v142\n"
    "v_mul_f32 v143, v219, v143\n"
    "v_mul_f32 v160, v219, v160\n"
    "v_mul_f32 v161, v219, v161\n"
    "v_mul_f32 v162, v219, v162\n"
    "v_mul_f32 v163, v219, v163\n"
    "v_mul_f32 v180, v219, v180\n"
    "v_mul_f32 v181, v219, v181\n"
    "v_mul_f32 v182, v219, v182\n"
    "v_mul_f32 v183, v219, v183\n"
    "v_mul_f32 v200, v219, v200\n"
    "v_mul_f32 v201, v219, v201\n"
    "v_mul_f32 v202, v219, v202\n"
    "v_mul_f32 v203, v219, v203\n"
    "v_accvgpr_read_b32 v22, a12\n"
    "v_mul_f32 v22, v219, v22\n"
    "v_accvgpr_write_b32 a12, v22\n"
    "v_accvgpr_read_b32 v23, a13\n"
    "v_mul_f32 v23, v219, v23\n"
    "v_accvgpr_write_b32 a13, v23\n"
    "v_accvgpr_read_b32 v22, a14\n"
    "v_mul_f32 v22, v219, v22\n"
    "v_accvgpr_write_b32 a14, v22\n"
    "v_accvgpr_read_b32 v23, a15\n"
    "v_mul_f32 v23, v219, v23\n"
    "v_accvgpr_write_b32 a15, v23\n"
    "v_accvgpr_read_b32 v22, a32\n"
    "v_mul_f32 v22, v219, v22\n"
    "v_accvgpr_write_b32 a32, v22\n"
    "v_accvgpr_read_b32 v23, a33\n"
    "v_mul_f32 v23, v219, v23\n"
    "v_accvgpr_write_b32 a33, v23\n"
    "v_accvgpr_read_b32 v22, a34\n"
    "v_mul_f32 v22, v219, v22\n"
    "v_accvgpr_write_b32 a34, v22\n"
    "v_accvgpr_read_b32 v23, a35\n"
    "v_mul_f32 v23, v219, v23\n"
    "v_accvgpr_write_b32 a35, v23\n"
    "v_accvgpr_read_b32 v22, a52\n"
    "v_mul_f32 v22, v219, v22\n"
    "v_accvgpr_write_b32 a52, v22\n"
    "v_accvgpr_read_b32 v23, a53\n"
    "v_mul_f32 v23, v219, v23\n"
    "v_accvgpr_write_b32 a53, v23\n"
    "v_accvgpr_read_b32 v22, a54\n"
    "v_mul_f32 v22, v219, v22\n"
    "v_accvgpr_write_b32 a54, v22\n"
    "v_accvgpr_read_b32 v23, a55\n"
    "v_mul_f32 v23, v219, v23\n"
    "v_accvgpr_write_b32 a55, v23\n"
    "v_accvgpr_read_b32 v22, a72\n"
    "v_mul_f32 v22, v219, v22\n"
    "v_accvgpr_write_b32 a72, v22\n"
    "v_accvgpr_read_b32 v23, a73\n"
    "v_mul_f32 v23, v219, v23\n"
    "v_accvgpr_write_b32 a73, v23\n"
    "v_accvgpr_read_b32 v22, a74\n"
    "v_mul_f32 v22, v219, v22\n"
    "v_accvgpr_write_b32 a74, v22\n"
    "v_accvgpr_read_b32 v23, a75\n"
    "v_mul_f32 v23, v219, v23\n"
    "v_accvgpr_write_b32 a75, v23\n"
    "v_accvgpr_read_b32 v22, a236\n"
    "v_mul_f32 v22, v219, v22\n"
    "v_accvgpr_write_b32 a236, v22\n"
    "v_accvgpr_read_b32 v23, a237\n"
    "v_mul_f32 v23, v219, v23\n"
    "v_accvgpr_write_b32 a237, v23\n"
    "v_accvgpr_read_b32 v22, a238\n"
    "v_mul_f32 v22, v219, v22\n"
    "v_accvgpr_write_b32 a238, v22\n"
    "v_accvgpr_read_b32 v23, a239\n"
    "v_mul_f32 v23, v219, v23\n"
    "v_accvgpr_write_b32 a239, v23\n"
    "v_mul_f32 v144, v220, v144\n"
    "v_mul_f32 v145, v220, v145\n"
    "v_mul_f32 v146, v220, v146\n"
    "v_mul_f32 v147, v220, v147\n"
    "v_mul_f32 v164, v220, v164\n"
    "v_mul_f32 v165, v220, v165\n"
    "v_mul_f32 v166, v220, v166\n"
    "v_mul_f32 v167, v220, v167\n"
    "v_mul_f32 v184, v220, v184\n"
    "v_mul_f32 v185, v220, v185\n"
    "v_mul_f32 v186, v220, v186\n"
    "v_mul_f32 v187, v220, v187\n"
    "v_mul_f32 v204, v220, v204\n"
    "v_mul_f32 v205, v220, v205\n"
    "v_mul_f32 v206, v220, v206\n"
    "v_mul_f32 v207, v220, v207\n"
    "v_accvgpr_read_b32 v22, a16\n"
    "v_mul_f32 v22, v220, v22\n"
    "v_accvgpr_write_b32 a16, v22\n"
    "v_accvgpr_read_b32 v23, a17\n"
    "v_mul_f32 v23, v220, v23\n"
    "v_accvgpr_write_b32 a17, v23\n"
    "v_accvgpr_read_b32 v22, a18\n"
    "v_mul_f32 v22, v220, v22\n"
    "v_accvgpr_write_b32 a18, v22\n"
    "v_accvgpr_read_b32 v23, a19\n"
    "v_mul_f32 v23, v220, v23\n"
    "v_accvgpr_write_b32 a19, v23\n"
    "v_accvgpr_read_b32 v22, a36\n"
    "v_mul_f32 v22, v220, v22\n"
    "v_accvgpr_write_b32 a36, v22\n"
    "v_accvgpr_read_b32 v23, a37\n"
    "v_mul_f32 v23, v220, v23\n"
    "v_accvgpr_write_b32 a37, v23\n"
    "v_accvgpr_read_b32 v22, a38\n"
    "v_mul_f32 v22, v220, v22\n"
    "v_accvgpr_write_b32 a38, v22\n"
    "v_accvgpr_read_b32 v23, a39\n"
    "v_mul_f32 v23, v220, v23\n"
    "v_accvgpr_write_b32 a39, v23\n"
    "v_accvgpr_read_b32 v22, a56\n"
    "v_mul_f32 v22, v220, v22\n"
    "v_accvgpr_write_b32 a56, v22\n"
    "v_accvgpr_read_b32 v23, a57\n"
    "v_mul_f32 v23, v220, v23\n"
    "v_accvgpr_write_b32 a57, v23\n"
    "v_accvgpr_read_b32 v22, a58\n"
    "v_mul_f32 v22, v220, v22\n"
    "v_accvgpr_write_b32 a58, v22\n"
    "v_accvgpr_read_b32 v23, a59\n"
    "v_mul_f32 v23, v220, v23\n"
    "v_accvgpr_write_b32 a59, v23\n"
    "v_accvgpr_read_b32 v22, a76\n"
    "v_mul_f32 v22, v220, v22\n"
    "v_accvgpr_write_b32 a76, v22\n"
    "v_accvgpr_read_b32 v23, a77\n"
    "v_mul_f32 v23, v220, v23\n"
    "v_accvgpr_write_b32 a77, v23\n"
    "v_accvgpr_read_b32 v22, a78\n"
    "v_mul_f32 v22, v220, v22\n"
    "v_accvgpr_write_b32 a78, v22\n"
    "v_accvgpr_read_b32 v23, a79\n"
    "v_mul_f32 v23, v220, v23\n"
    "v_accvgpr_write_b32 a79, v23\n"
    "v_accvgpr_read_b32 v22, a240\n"
    "v_mul_f32 v22, v220, v22\n"
    "v_accvgpr_write_b32 a240, v22\n"
    "v_accvgpr_read_b32 v23, a241\n"
    "v_mul_f32 v23, v220, v23\n"
    "v_accvgpr_write_b32 a241, v23\n"
    "v_accvgpr_read_b32 v22, a242\n"
    "v_mul_f32 v22, v220, v22\n"
    "v_accvgpr_write_b32 a242, v22\n"
    "v_accvgpr_read_b32 v23, a243\n"
    "v_mul_f32 v23, v220, v23\n"
    "v_accvgpr_write_b32 a243, v23\n"
    "LSLOWX%=:\n"
    "s_nop 3\n"
    "s_cmp_eq_u32 s34, 0\n"
    "s_cbranch_scc1 LPHBA%=\n"
    "s_branch LPHBB%=\n"
    "LS4A%=:\n"
    "s_nop 3\n"
    "v_max3_f32 v22, v96, v97, v98\n"
    "v_max3_f32 v23, v100, v101, v102\n"
    "v_max3_f32 v22, v22, v99, v103\n"
    "v_max_f32 v22, v22, v23\n"
    "v_mov_b32 v23, v22\n"
    "s_nop 1\n"
    "v_permlane32_swap_b32 v22, v23\n"
    "v_max_f32 v22, v22, v23\n"
    "v_mov_b32 v23, v22\n"
    "s_nop 1\n"
    "v_permlane16_swap_b32 v22, v23\n"
    "v_max3_f32 v22, v22, v23, 0\n"
    "v_sub_f32 v96, v96, v22\n"
    "v_sub_f32 v97, v97, v22\n"
    "v_sub_f32 v98, v98, v22\n"
    "v_sub_f32 v99, v99, v22\n"
    "v_sub_f32 v100, v100, v22\n"
    "v_sub_f32 v101, v101, v22\n"
    "v_sub_f32 v102, v102, v22\n"
    "v_sub_f32 v103, v103, v22\n"
    "v_sub_f32 v40, v40, v22\n"
    "v_sub_f32 v41, v41, v22\n"
    "v_sub_f32 v42, v42, v22\n"
    "v_sub_f32 v43, v43, v22\n"
    "v_max_f32 v23, 0, v22\n"
    "v_exp_f32 v220, -v23\n"
    "s_nop 0\n"
    "v_mul_f32 v144, v220, v144\n"
    "v_mul_f32 v145, v220, v145\n"
    "v_mul_f32 v146, v220, v146\n"
    "v_mul_f32 v147, v220, v147\n"
    "v_mul_f32 v164, v220, v164\n"
    "v_mul_f32 v165, v220, v165\n"
    "v_mul_f32 v166, v220, v166\n"
    "v_mul_f32 v167, v220, v167\n"
    "v_mul_f32 v184, v220, v184\n"
    "v_mul_f32 v185, v220, v185\n"
    "v_mul_f32 v186, v220, v186\n"
    "v_mul_f32 v187, v220, v187\n"
    "v_mul_f32 v204, v220, v204\n"
    "v_mul_f32 v205, v220, v205\n"
    "v_mul_f32 v206, v220, v206\n"
    "v_mul_f32 v207, v220, v207\n"
    "v_accvgpr_read_b32 v22, a16\n"
    "v_mul_f32 v22, v220, v22\n"
    "v_accvgpr_write_b32 a16, v22\n"
    "v_accvgpr_read_b32 v23, a17\n"
    "v_mul_f32 v23, v220, v23\n"
    "v_accvgpr_write_b32 a17, v23\n"
    "v_accvgpr_read_b32 v22, a18\n"
    "v_mul_f32 v22, v220, v22\n"
    "v_accvgpr_write_b32 a18, v22\n"
    "v_accvgpr_read_b32 v23, a19\n"
    "v_mul_f32 v23, v220, v23\n"
    "v_accvgpr_write_b32 a19, v23\n"
    "v_accvgpr_read_b32 v22, a36\n"
    "v_mul_f32 v22, v220, v22\n"
    "v_accvgpr_write_b32 a36, v22\n"
    "v_accvgpr_read_b32 v23, a37\n"
    "v_mul_f32 v23, v220, v23\n"
    "v_accvgpr_write_b32 a37, v23\n"
    "v_accvgpr_read_b32 v22, a38\n"
    "v_mul_f32 v22, v220, v22\n"
    "v_accvgpr_write_b32 a38, v22\n"
    "v_accvgpr_read_b32 v23, a39\n"
    "v_mul_f32 v23, v220, v23\n"
    "v_accvgpr_write_b32 a39, v23\n"
    "v_accvgpr_read_b32 v22, a56\n"
    "v_mul_f32 v22, v220, v22\n"
    "v_accvgpr_write_b32 a56, v22\n"
    "v_accvgpr_read_b32 v23, a57\n"
    "v_mul_f32 v23, v220, v23\n"
    "v_accvgpr_write_b32 a57, v23\n"
    "v_accvgpr_read_b32 v22, a58\n"
    "v_mul_f32 v22, v220, v22\n"
    "v_accvgpr_write_b32 a58, v22\n"
    "v_accvgpr_read_b32 v23, a59\n"
    "v_mul_f32 v23, v220, v23\n"
    "v_accvgpr_write_b32 a59, v23\n"
    "v_accvgpr_read_b32 v22, a76\n"
    "v_mul_f32 v22, v220, v22\n"
    "v_accvgpr_write_b32 a76, v22\n"
    "v_accvgpr_read_b32 v23, a77\n"
    "v_mul_f32 v23, v220, v23\n"
    "v_accvgpr_write_b32 a77, v23\n"
    "v_accvgpr_read_b32 v22, a78\n"
    "v_mul_f32 v22, v220, v22\n"
    "v_accvgpr_write_b32 a78, v22\n"
    "v_accvgpr_read_b32 v23, a79\n"
    "v_mul_f32 v23, v220, v23\n"
    "v_accvgpr_write_b32 a79, v23\n"
    "v_accvgpr_read_b32 v22, a240\n"
    "v_mul_f32 v22, v220, v22\n"
    "v_accvgpr_write_b32 a240, v22\n"
    "v_accvgpr_read_b32 v23, a241\n"
    "v_mul_f32 v23, v220, v23\n"
    "v_accvgpr_write_b32 a241, v23\n"
    "v_accvgpr_read_b32 v22, a242\n"
    "v_mul_f32 v22, v220, v22\n"
    "v_accvgpr_write_b32 a242, v22\n"
    "v_accvgpr_read_b32 v23, a243\n"
    "v_mul_f32 v23, v220, v23\n"
    "v_accvgpr_write_b32 a243, v23\n"
    "s_nop 3\n"
    "s_branch LS4XA%=\n"
    "LS4B%=:\n"
    "s_nop 3\n"
    "v_max3_f32 v22, v96, v97, v98\n"
    "v_max3_f32 v23, v100, v101, v102\n"
    "v_max3_f32 v22, v22, v99, v103\n"
    "v_max_f32 v22, v22, v23\n"
    "v_mov_b32 v23, v22\n"
    "s_nop 1\n"
    "v_permlane32_swap_b32 v22, v23\n"
    "v_max_f32 v22, v22, v23\n"
    "v_mov_b32 v23, v22\n"
    "s_nop 1\n"
    "v_permlane16_swap_b32 v22, v23\n"
    "v_max3_f32 v22, v22, v23, 0\n"
    "v_sub_f32 v96, v96, v22\n"
    "v_sub_f32 v97, v97, v22\n"
    "v_sub_f32 v98, v98, v22\n"
    "v_sub_f32 v99, v99, v22\n"
    "v_sub_f32 v100, v100, v22\n"
    "v_sub_f32 v101, v101, v22\n"
    "v_sub_f32 v102, v102, v22\n"
    "v_sub_f32 v103, v103, v22\n"
    "v_sub_f32 v40, v40, v22\n"
    "v_sub_f32 v41, v41, v22\n"
    "v_sub_f32 v42, v42, v22\n"
    "v_sub_f32 v43, v43, v22\n"
    "v_max_f32 v23, 0, v22\n"
    "v_exp_f32 v220, -v23\n"
    "s_nop 0\n"
    "v_mul_f32 v144, v220, v144\n"
    "v_mul_f32 v145, v220, v145\n"
    "v_mul_f32 v146, v220, v146\n"
    "v_mul_f32 v147, v220, v147\n"
    "v_mul_f32 v164, v220, v164\n"
    "v_mul_f32 v165, v220, v165\n"
    "v_mul_f32 v166, v220, v166\n"
    "v_mul_f32 v167, v220, v167\n"
    "v_mul_f32 v184, v220, v184\n"
    "v_mul_f32 v185, v220, v185\n"
    "v_mul_f32 v186, v220, v186\n"
    "v_mul_f32 v187, v220, v187\n"
    "v_mul_f32 v204, v220, v204\n"
    "v_mul_f32 v205, v220, v205\n"
    "v_mul_f32 v206, v220, v206\n"
    "v_mul_f32 v207, v220, v207\n"
    "v_accvgpr_read_b32 v22, a16\n"
    "v_mul_f32 v22, v220, v22\n"
    "v_accvgpr_write_b32 a16, v22\n"
    "v_accvgpr_read_b32 v23, a17\n"
    "v_mul_f32 v23, v220, v23\n"
    "v_accvgpr_write_b32 a17, v23\n"
    "v_accvgpr_read_b32 v22, a18\n"
    "v_mul_f32 v22, v220, v22\n"
    "v_accvgpr_write_b32 a18, v22\n"
    "v_accvgpr_read_b32 v23, a19\n"
    "v_mul_f32 v23, v220, v23\n"
    "v_accvgpr_write_b32 a19, v23\n"
    "v_accvgpr_read_b32 v22, a36\n"
    "v_mul_f32 v22, v220, v22\n"
    "v_accvgpr_write_b32 a36, v22\n"
    "v_accvgpr_read_b32 v23, a37\n"
    "v_mul_f32 v23, v220, v23\n"
    "v_accvgpr_write_b32 a37, v23\n"
    "v_accvgpr_read_b32 v22, a38\n"
    "v_mul_f32 v22, v220, v22\n"
    "v_accvgpr_write_b32 a38, v22\n"
    "v_accvgpr_read_b32 v23, a39\n"
    "v_mul_f32 v23, v220, v23\n"
    "v_accvgpr_write_b32 a39, v23\n"
    "v_accvgpr_read_b32 v22, a56\n"
    "v_mul_f32 v22, v220, v22\n"
    "v_accvgpr_write_b32 a56, v22\n"
    "v_accvgpr_read_b32 v23, a57\n"
    "v_mul_f32 v23, v220, v23\n"
    "v_accvgpr_write_b32 a57, v23\n"
    "v_accvgpr_read_b32 v22, a58\n"
    "v_mul_f32 v22, v220, v22\n"
    "v_accvgpr_write_b32 a58, v22\n"
    "v_accvgpr_read_b32 v23, a59\n"
    "v_mul_f32 v23, v220, v23\n"
    "v_accvgpr_write_b32 a59, v23\n"
    "v_accvgpr_read_b32 v22, a76\n"
    "v_mul_f32 v22, v220, v22\n"
    "v_accvgpr_write_b32 a76, v22\n"
    "v_accvgpr_read_b32 v23, a77\n"
    "v_mul_f32 v23, v220, v23\n"
    "v_accvgpr_write_b32 a77, v23\n"
    "v_accvgpr_read_b32 v22, a78\n"
    "v_mul_f32 v22, v220, v22\n"
    "v_accvgpr_write_b32 a78, v22\n"
    "v_accvgpr_read_b32 v23, a79\n"
    "v_mul_f32 v23, v220, v23\n"
    "v_accvgpr_write_b32 a79, v23\n"
    "v_accvgpr_read_b32 v22, a240\n"
    "v_mul_f32 v22, v220, v22\n"
    "v_accvgpr_write_b32 a240, v22\n"
    "v_accvgpr_read_b32 v23, a241\n"
    "v_mul_f32 v23, v220, v23\n"
    "v_accvgpr_write_b32 a241, v23\n"
    "v_accvgpr_read_b32 v22, a242\n"
    "v_mul_f32 v22, v220, v22\n"
    "v_accvgpr_write_b32 a242, v22\n"
    "v_accvgpr_read_b32 v23, a243\n"
    "v_mul_f32 v23, v220, v23\n"
    "v_accvgpr_write_b32 a243, v23\n"
    "s_nop 3\n"
    "s_branch LS4XB%=\n"
    "LS4L%=:\n"
    "s_nop 3\n"
    "v_max3_f32 v22, v96, v97, v98\n"
    "v_max3_f32 v23, v100, v101, v102\n"
    "v_max3_f32 v22, v22, v99, v103\n"
    "v_max_f32 v22, v22, v23\n"
    "v_mov_b32 v23, v22\n"
    "s_nop 1\n"
    "v_permlane32_swap_b32 v22, v23\n"
    "v_max_f32 v22, v22, v23\n"
    "v_mov_b32 v23, v22\n"
    "s_nop 1\n"
    "v_permlane16_swap_b32 v22, v23\n"
    "v_max3_f32 v22, v22, v23, 0\n"
    "v_sub_f32 v96, v96, v22\n"
    "v_sub_f32 v97, v97, v22\n"
    "v_sub_f32 v98, v98, v22\n"
    "v_sub_f32 v99, v99, v22\n"
    "v_sub_f32 v100, v100, v22\n"
    "v_sub_f32 v101, v101, v22\n"
    "v_sub_f32 v102, v102, v22\n"
    "v_sub_f32 v103, v103, v22\n"
    "v_sub_f32 v40, v40, v22\n"
    "v_sub_f32 v41, v41, v22\n"
    "v_sub_f32 v42, v42, v22\n"
    "v_sub_f32 v43, v43, v22\n"
    "v_max_f32 v23, 0, v22\n"
    "v_exp_f32 v220, -v23\n"
    "s_nop 0\n"
    "v_mul_f32 v144, v220, v144\n"
    "v_mul_f32 v145, v220, v145\n"
    "v_mul_f32 v146, v220, v146\n"
    "v_mul_f32 v147, v220, v147\n"
    "v_mul_f32 v164, v220, v164\n"
    "v_mul_f32 v165, v220, v165\n"
    "v_mul_f32 v166, v220, v166\n"
    "v_mul_f32 v167, v220, v167\n"
    "v_mul_f32 v184, v220, v184\n"
    "v_mul_f32 v185, v220, v185\n"
    "v_mul_f32 v186, v220, v186\n"
    "v_mul_f32 v187, v220, v187\n"
    "v_mul_f32 v204, v220, v204\n"
    "v_mul_f32 v205, v220, v205\n"
    "v_mul_f32 v206, v220, v206\n"
    "v_mul_f32 v207, v220, v207\n"
    "v_accvgpr_read_b32 v22, a16\n"
    "v_mul_f32 v22, v220, v22\n"
    "v_accvgpr_write_b32 a16, v22\n"
    "v_accvgpr_read_b32 v23, a17\n"
    "v_mul_f32 v23, v220, v23\n"
    "v_accvgpr_write_b32 a17, v23\n"
    "v_accvgpr_read_b32 v22, a18\n"
    "v_mul_f32 v22, v220, v22\n"
    "v_accvgpr_write_b32 a18, v22\n"
    "v_accvgpr_read_b32 v23, a19\n"
    "v_mul_f32 v23, v220, v23\n"
    "v_accvgpr_write_b32 a19, v23\n"
    "v_accvgpr_read_b32 v22, a36\n"
    "v_mul_f32 v22, v220, v22\n"
    "v_accvgpr_write_b32 a36, v22\n"
    "v_accvgpr_read_b32 v23, a37\n"
    "v_mul_f32 v23, v220, v23\n"
    "v_accvgpr_write_b32 a37, v23\n"
    "v_accvgpr_read_b32 v22, a38\n"
    "v_mul_f32 v22, v220, v22\n"
    "v_accvgpr_write_b32 a38, v22\n"
    "v_accvgpr_read_b32 v23, a39\n"
    "v_mul_f32 v23, v220, v23\n"
    "v_accvgpr_write_b32 a39, v23\n"
    "v_accvgpr_read_b32 v22, a56\n"
    "v_mul_f32 v22, v220, v22\n"
    "v_accvgpr_write_b32 a56, v22\n"
    "v_accvgpr_read_b32 v23, a57\n"
    "v_mul_f32 v23, v220, v23\n"
    "v_accvgpr_write_b32 a57, v23\n"
    "v_accvgpr_read_b32 v22, a58\n"
    "v_mul_f32 v22, v220, v22\n"
    "v_accvgpr_write_b32 a58, v22\n"
    "v_accvgpr_read_b32 v23, a59\n"
    "v_mul_f32 v23, v220, v23\n"
    "v_accvgpr_write_b32 a59, v23\n"
    "v_accvgpr_read_b32 v22, a76\n"
    "v_mul_f32 v22, v220, v22\n"
    "v_accvgpr_write_b32 a76, v22\n"
    "v_accvgpr_read_b32 v23, a77\n"
    "v_mul_f32 v23, v220, v23\n"
    "v_accvgpr_write_b32 a77, v23\n"
    "v_accvgpr_read_b32 v22, a78\n"
    "v_mul_f32 v22, v220, v22\n"
    "v_accvgpr_write_b32 a78, v22\n"
    "v_accvgpr_read_b32 v23, a79\n"
    "v_mul_f32 v23, v220, v23\n"
    "v_accvgpr_write_b32 a79, v23\n"
    "v_accvgpr_read_b32 v22, a240\n"
    "v_mul_f32 v22, v220, v22\n"
    "v_accvgpr_write_b32 a240, v22\n"
    "v_accvgpr_read_b32 v23, a241\n"
    "v_mul_f32 v23, v220, v23\n"
    "v_accvgpr_write_b32 a241, v23\n"
    "v_accvgpr_read_b32 v22, a242\n"
    "v_mul_f32 v22, v220, v22\n"
    "v_accvgpr_write_b32 a242, v22\n"
    "v_accvgpr_read_b32 v23, a243\n"
    "v_mul_f32 v23, v220, v23\n"
    "v_accvgpr_write_b32 a243, v23\n"
    "s_nop 3\n"
    "s_branch LS4XL%=\n"
    "LLAST%=:\n"
    "v_add_u32 v13, s56, v211\n"
    "v_xor_b32 v14, 0x20, v13\n"
    "v_xor_b32 v15, 0x40, v13\n"
    "v_xor_b32 v16, 0x60, v13\n"
    "s_waitcnt lgkmcnt(0)\n"
    "v_mfma_f32_16x16x32_bf16 a[0:3], a[208:211], v[44:47], a[0:3]\n"
    "s_waitcnt vmcnt(8)\n"
    "v_exp_f32 v80, v80\n"
    "v_max_f32 v22, v96, v97\n"
    "v_mfma_f32_16x16x32_bf16 a[20:23], a[212:215], v[44:47], a[20:23]\n"
    "ds_read_b64_tr_b16 a[160:161], v13 offset:24576\n"
    "ds_read_b64_tr_b16 a[162:163], v13 offset:26624\n"
    "v_mfma_f32_16x16x32_bf16 a[40:43], a[216:219], v[44:47], a[40:43]\n"
    "ds_read_b64_tr_b16 a[164:165], v14 offset:24576\n"
    "v_max3_f32 v22, v22, v98, v99\n"
    "v_max3_f32 v22, v22, v100, v101\n"
    "v_mfma_f32_16x16x32_bf16 a[60:63], a[220:223], v[44:47], a[60:63]\n"
    "ds_read_b64_tr_b16 a[166:167], v14 offset:26624\n"
    "v_max3_f32 v22, v22, v102, v103\n"
    "v_cmp_lt_f32 vcc, s63, v22\n"
    "v_mfma_f32_16x16x32_bf16 a[224:227], v[104:107], v[44:47], a[224:227]\n"
    "s_add_u32 m0, s56, 32768\n"
    "s_cbranch_vccnz LS4L%=\n"
    "LS4XL%=:\n"
    "v_exp_f32 v81, v81\n"
    "v_mfma_f32_16x16x32_bf16 v[128:131], a[192:195], v[44:47], v[128:131]\n"
    "buffer_load_dwordx4 v222, s[44:47], s66 offen offset:0 lds\n"
    "ds_read_b64_tr_b16 a[168:169], v15 offset:24576\n"
    "v_mfma_f32_16x16x32_bf16 v[148:151], a[196:199], v[44:47], v[148:151]\n"
    "ds_read_b64_tr_b16 a[170:171], v15 offset:26624\n"
    "buffer_load_dwordx4 v222, s[44:47], s66 offen offset:1024 lds\n"
    "v_mfma_f32_16x16x32_bf16 v[168:171], a[200:203], v[44:47], v[168:171]\n"
    "ds_write_b128 v111, a[0:3] offset:0\n"
    "ds_write_b128 v111, a[20:23] offset:1024\n"
    "v_mfma_f32_16x16x32_bf16 v[188:191], a[204:207], v[44:47], v[188:191]\n"
    "ds_write_b128 v111, a[40:43] offset:2048\n"
    "v_cvt_pk_bf16_f32 v52, v80, v81\n"
    "v_mfma_f32_16x16x32_bf16 a[4:7], a[208:211], v[48:51], a[4:7]\n"
    "ds_write_b128 v111, a[60:63] offset:3072\n"
    "v_exp_f32 v82, v82\n"
    "v_mfma_f32_16x16x32_bf16 a[24:27], a[212:215], v[48:51], a[24:27]\n"
    "buffer_load_dwordx4 v223, s[44:47], s66 offen offset:2048 lds\n"
    "v_exp_f32 v83, v83\n"
    "v_mfma_f32_16x16x32_bf16 a[44:47], a[216:219], v[48:51], a[44:47]\n"
    "ds_read_b64_tr_b16 a[172:173], v16 offset:24576\n"
    "ds_read_b64_tr_b16 a[174:175], v16 offset:26624\n"
    "v_mfma_f32_16x16x32_bf16 a[64:67], a[220:223], v[48:51], a[64:67]\n"
    "v_cvt_pk_bf16_f32 v53, v82, v83\n"
    "v_exp_f32 v84, v84\n"
    "v_mfma_f32_16x16x32_bf16 a[228:231], v[104:107], v[48:51], a[228:231]\n"
    "ds_read_b64_tr_b16 a[176:177], v13 offset:28672\n"
    "v_exp_f32 v85, v85\n"
    "v_mfma_f32_16x16x32_bf16 v[132:135], a[192:195], v[48:51], v[132:135]\n"
    "ds_read_b64_tr_b16 a[178:179], v13 offset:30720\n"
    "v_exp_f32 v86, v86\n"
    "v_mfma_f32_16x16x32_bf16 v[152:155], a[196:199], v[48:51], v[152:155]\n"
    "ds_read_b64_tr_b16 a[180:181], v14 offset:28672\n"
    "v_exp_f32 v87, v87\n"
    "v_mfma_f32_16x16x32_bf16 v[172:175], a[200:203], v[48:51], v[172:175]\n"
    "ds_read_b64_tr_b16 a[182:183], v14 offset:30720\n"
    "v_cvt_pk_bf16_f32 v54, v84, v85\n"
    "v_cvt_pk_bf16_f32 v55, v86, v87\n"
    "v_mfma_f32_16x16x32_bf16 v[192:195], a[204:207], v[48:51], v[192:195]\n"
    "ds_write_b128 v111, a[4:7] offset:4096\n"
    "v_exp_f32 v88, v88\n"
    "v_mfma_f32_16x16x32_bf16 a[8:11], a[208:211], v[52:55], a[8:11]\n"
    "ds_write_b128 v111, a[24:27] offset:5120\n"
    "v_exp_f32 v89, v89\n"
    "v_mfma_f32_16x16x32_bf16 a[28:31], a[212:215], v[52:55], a[28:31]\n"
    "ds_write_b128 v111, a[44:47] offset:6144\n"
    "buffer_load_dwordx4 v223, s[44:47], s66 offen offset:3072 lds\n"
    "v_mfma_f32_16x16x32_bf16 a[48:51], a[216:219], v[52:55], a[48:51]\n"
    "ds_write_b128 v111, a[64:67] offset:7168\n"
    "v_exp_f32 v90, v90\n"
    "v_exp_f32 v91, v91\n"
    "v_mfma_f32_16x16x32_bf16 a[68:71], a[220:223], v[52:55], a[68:71]\n"
    "ds_read_b64_tr_b16 a[184:185], v15 offset:28672\n"
    "v_cvt_pk_bf16_f32 v56, v88, v89\n"
    "v_cvt_pk_bf16_f32 v57, v90, v91\n"
    "v_mfma_f32_16x16x32_bf16 a[232:235], v[104:107], v[52:55], a[232:235]\n"
    "ds_read_b64_tr_b16 a[186:187], v15 offset:30720\n"
    "v_exp_f32 v92, v92\n"
    "v_mfma_f32_16x16x32_bf16 v[136:139], a[192:195], v[52:55], v[136:139]\n"
    "ds_read_b64_tr_b16 a[188:189], v16 offset:28672\n"
    "v_exp_f32 v93, v93\n"
    "v_mfma_f32_16x16x32_bf16 v[156:159], a[196:199], v[52:55], v[156:159]\n"
    "s_add_u32 m0, s56, 36864\n"
    "v_exp_f32 v94, v94\n"
    "v_exp_f32 v95, v95\n"
    "v_mfma_f32_16x16x32_bf16 v[176:179], a[200:203], v[52:55], v[176:179]\n"
    "buffer_load_dwordx4 v222, s[44:47], s67 offen offset:0 lds\n"
    "v_cvt_pk_bf16_f32 v58, v92, v93\n"
    "v_cvt_pk_bf16_f32 v59, v94, v95\n"
    "v_mfma_f32_16x16x32_bf16 v[196:199], a[204:207], v[52:55], v[196:199]\n"
    "ds_read_b64_tr_b16 a[190:191], v16 offset:30720\n"
    "v_exp_f32 v96, v96\n"
    "v_mfma_f32_16x16x32_bf16 a[12:15], a[208:211], v[56:59], a[12:15]\n"
    "buffer_load_dwordx4 v222, s[44:47], s67 offen offset:1024 lds\n"
    "v_accvgpr_read_b32 v25, a224\n"
    "v_accvgpr_read_b32 v29, a228\n"
    "v_mfma_f32_16x16x32_bf16 a[32:35], a[212:215], v[56:59], a[32:35]\n"
    "ds_write_b128 v111, a[8:11] offset:8192\n"
    "v_exp_f32 v97, v97\n"
    "v_mfma_f32_16x16x32_bf16 a[52:55], a[216:219], v[56:59], a[52:55]\n"
    "v_exp_f32 v98, v98\n"
    "v_exp_f32 v99, v99\n"
    "v_mfma_f32_16x16x32_bf16 a[72:75], a[220:223], v[56:59], a[72:75]\n"
    "ds_write_b128 v111, a[28:31] offset:9216\n"
    "v_cvt_pk_bf16_f32 v60, v96, v97\n"
    "v_cvt_pk_bf16_f32 v61, v98, v99\n"
    "v_mfma_f32_16x16x32_bf16 a[236:239], v[104:107], v[56:59], a[236:239]\n"
    "ds_write_b128 v111, a[48:51] offset:10240\n"
    "v_exp_f32 v100, v100\n"
    "v_mfma_f32_16x16x32_bf16 v[140:143], a[192:195], v[56:59], v[140:143]\n"
    "ds_write_b128 v111, a[68:71] offset:11264\n"
    "v_exp_f32 v101, v101\n"
    "v_mfma_f32_16x16x32_bf16 v[160:163], a[196:199], v[56:59], v[160:163]\n"
    "v_exp_f32 v102, v102\n"
    "v_exp_f32 v103, v103\n"
    "v_mfma_f32_16x16x32_bf16 v[180:183], a[200:203], v[56:59], v[180:183]\n"
    "buffer_load_dwordx4 v223, s[44:47], s67 offen offset:2048 lds\n"
    "v_cvt_pk_bf16_f32 v62, v100, v101\n"
    "v_cvt_pk_bf16_f32 v63, v102, v103\n"
    "v_mfma_f32_16x16x32_bf16 v[200:203], a[204:207], v[56:59], v[200:203]\n"
    "ds_write_b64 v116, v[24:25] offset:28672\n"
    "v_accvgpr_read_b32 v33, a232\n"
    "v_mfma_f32_16x16x32_bf16 a[16:19], a[208:211], v[60:63], a[16:19]\n"
    "ds_write_b64 v116, v[28:29] offset:29184\n"
    "v_accvgpr_read_b32 v37, a236\n"
    "v_mfma_f32_16x16x32_bf16 a[36:39], a[212:215], v[60:63], a[36:39]\n"
    "ds_write_b64 v116, v[32:33] offset:29696\n"
    "ds_write_b128 v111, a[12:15] offset:12288\n"
    "v_mfma_f32_16x16x32_bf16 a[56:59], a[216:219], v[60:63], a[56:59]\n"
    "buffer_load_dwordx4 v223, s[44:47], s67 offen offset:3072 lds\n"
    "ds_write_b128 v111, a[32:35] offset:13312\n"
    "v_mfma_f32_16x16x32_bf16 a[76:79], a[220:223], v[60:63], a[76:79]\n"
    "ds_write_b128 v111, a[52:55] offset:14336\n"
    "ds_write_b128 v111, a[72:75] offset:15360\n"
    "v_mfma_f32_16x16x32_bf16 a[240:243], v[104:107], v[60:63], a[240:243]\n"
    "ds_write_b64 v116, v[36:37] offset:30208\n"
    "v_mfma_f32_16x16x32_bf16 v[144:147], a[192:195], v[60:63], v[144:147]\n"
    "ds_write_b128 v111, a[16:19] offset:24576\n"
    "ds_write_b128 v111, a[36:39] offset:25600\n"
    "v_mfma_f32_16x16x32_bf16 v[164:167], a[196:199], v[60:63], v[164:167]\n"
    "ds_write_b128 v111, a[56:59] offset:26624\n"
    "ds_write_b128 v111, a[76:79] offset:27648\n"
    "v_mfma_f32_16x16x32_bf16 v[184:187], a[200:203], v[60:63], v[184:187]\n"
    "v_mfma_f32_16x16x32_bf16 v[204:207], a[204:207], v[60:63], v[204:207]\n"
    "v_accvgpr_read_b32 v41, a240\n"
    "ds_write_b64 v116, v[40:41] offset:30720\n"
    "s_branch LJOIN%=\n"
    "LEPIB%=:\n"
    "LEPIA%=:\n"
    "v_add_u32 v13, s56, v211\n"
    "v_xor_b32 v14, 0x20, v13\n"
    "v_xor_b32 v15, 0x40, v13\n"
    "v_xor_b32 v16, 0x60, v13\n"
    "s_nop 3\n"
    "ds_write_b128 v111, a[0:3] offset:0\n"
    "ds_write_b128 v111, a[20:23] offset:1024\n"
    "ds_write_b128 v111, a[40:43] offset:2048\n"
    "ds_write_b128 v111, a[60:63] offset:3072\n"
    "ds_write_b128 v111, a[4:7] offset:4096\n"
    "ds_write_b128 v111, a[24:27] offset:5120\n"
    "ds_write_b128 v111, a[44:47] offset:6144\n"
    "ds_write_b128 v111, a[64:67] offset:7168\n"
    "ds_write_b128 v111, a[8:11] offset:8192\n"
    "ds_write_b128 v111, a[28:31] offset:9216\n"
    "ds_write_b128 v111, a[48:51] offset:10240\n"
    "ds_write_b128 v111, a[68:71] offset:11264\n"
    "ds_write_b128 v111, a[12:15] offset:12288\n"
    "ds_write_b128 v111, a[32:35] offset:13312\n"
    "ds_write_b128 v111, a[52:55] offset:14336\n"
    "ds_write_b128 v111, a[72:75] offset:15360\n"
    "s_mov_b32 s44, s12\n"
    "s_and_b32 s45, s13, 0xffff\n"
    "s_mov_b32 s46, 261376\n"
    "s_mov_b32 s47, 0x20000\n"
    "v_lshlrev_b32 v123, 3, v0\n"
    "v_add_u32 v116, s56, v123\n"
    "v_add_u32 v117, s57, v123\n"
    "v_lshlrev_b32 v123, 4, v0\n"
    "s_lshl_b32 s69, s20, 1\n"
    "s_add_u32 s69, s69, s34\n"
    "s_lshl_b32 s69, s69, 4\n"
    "s_add_u32 s69, s69, s21\n"
    "s_mul_i32 s66, s69, 81920\n"
    "s_mul_i32 s67, %2, 20480\n"
    "s_add_u32 s66, s66, s67\n"
    "s_add_u32 s52, s14, s66\n"
    "s_addc_u32 s53, s15, 0\n"
    "s_and_b32 s53, s53, 0xffff\n"
    "s_mov_b32 s54, 20480\n"
    "s_mov_b32 s55, 0x20000\n"
    "s_add_u32 s66, %2, 0\n"
    "s_and_b32 s66, s66, 3\n"
    "s_xor_b32 s66, s66, 1\n"
    "s_mul_i32 s66, s66, 40960\n"
    "v_add_u32 v118, s66, v123\n"
    "s_add_u32 s66, %2, 1\n"
    "s_and_b32 s66, s66, 3\n"
    "s_xor_b32 s66, s66, 1\n"
    "s_mul_i32 s66, s66, 40960\n"
    "v_add_u32 v119, s66, v123\n"
    "s_add_u32 s66, %2, 2\n"
    "s_and_b32 s66, s66, 3\n"
    "s_xor_b32 s66, s66, 1\n"
    "s_mul_i32 s66, s66, 40960\n"
    "v_add_u32 v120, s66, v123\n"
    "s_add_u32 s66, %2, 3\n"
    "s_and_b32 s66, s66, 3\n"
    "s_xor_b32 s66, s66, 1\n"
    "s_mul_i32 s66, s66, 40960\n"
    "v_add_u32 v121, s66, v123\n"
    "s_waitcnt vmcnt(8)\n"
    "ds_read_b64_tr_b16 a[160:161], v13 offset:24576\n"
    "ds_read_b64_tr_b16 a[162:163], v13 offset:26624\n"
    "ds_read_b64_tr_b16 a[164:165], v14 offset:24576\n"
    "ds_read_b64_tr_b16 a[166:167], v14 offset:26624\n"
    "ds_read_b64_tr_b16 a[168:169], v15 offset:24576\n"
    "ds_read_b64_tr_b16 a[170:171], v15 offset:26624\n"
    "ds_read_b64_tr_b16 a[172:173], v16 offset:24576\n"
    "ds_read_b64_tr_b16 a[174:175], v16 offset:26624\n"
    "ds_read_b64_tr_b16 a[176:177], v13 offset:28672\n"
    "ds_read_b64_tr_b16 a[178:179], v13 offset:30720\n"
    "ds_read_b64_tr_b16 a[180:181], v14 offset:28672\n"
    "ds_read_b64_tr_b16 a[182:183], v14 offset:30720\n"
    "ds_read_b64_tr_b16 a[184:185], v15 offset:28672\n"
    "ds_read_b64_tr_b16 a[186:187], v15 offset:30720\n"
    "ds_read_b64_tr_b16 a[188:189], v16 offset:28672\n"
    "ds_read_b64_tr_b16 a[190:191], v16 offset:30720\n"
    "ds_write_b128 v111, a[16:19] offset:24576\n"
    "ds_write_b128 v111, a[36:39] offset:25600\n"
    "ds_write_b128 v111, a[56:59] offset:26624\n"
    "ds_write_b128 v111, a[76:79] offset:27648\n"
    "v_accvgpr_read_b32 v25, a224\n"
    "ds_write_b64 v116, v[24:25] offset:28672\n"
    "v_accvgpr_read_b32 v29, a228\n"
    "ds_write_b64 v116, v[28:29] offset:29184\n"
    "v_accvgpr_read_b32 v33, a232\n"
    "ds_write_b64 v116, v[32:33] offset:29696\n"
    "v_accvgpr_read_b32 v37, a236\n"
    "ds_write_b64 v116, v[36:37] offset:30208\n"
    "v_accvgpr_read_b32 v41, a240\n"
    "ds_write_b64 v116, v[40:41] offset:30720\n"
    "LJOIN%=:\n"
    "s_waitcnt lgkmcnt(0)\n"
    "s_barrier\n"
    "ds_read_b64 v[26:27], v117 offset:28672\n"
    "ds_read_b64 v[30:31], v117 offset:29184\n"
    "ds_read_b64 v[34:35], v117 offset:29696\n"
    "ds_read_b64 v[38:39], v117 offset:30208\n"
    "ds_read_b64 v[42:43], v117 offset:30720\n"
    "ds_read_b128 v[44:47], v112 offset:0\n"
    "ds_read_b128 v[64:67], v112 offset:1024\n"
    "ds_read_b128 v[84:87], v112 offset:2048\n"
    "ds_read_b128 v[224:227], v112 offset:3072\n"
    "ds_read_b128 v[48:51], v112 offset:4096\n"
    "ds_read_b128 v[68:71], v112 offset:5120\n"
    "ds_read_b128 v[88:91], v112 offset:6144\n"
    "ds_read_b128 v[228:231], v112 offset:7168\n"
    "ds_read_b128 v[52:55], v112 offset:8192\n"
    "ds_read_b128 v[72:75], v112 offset:9216\n"
    "ds_read_b128 v[92:95], v112 offset:10240\n"
    "ds_read_b128 v[232:235], v112 offset:11264\n"
    "ds_read_b128 v[56:59], v112 offset:12288\n"
    "ds_read_b128 v[76:79], v112 offset:13312\n"
    "ds_read_b128 v[96:99], v112 offset:14336\n"
    "ds_read_b128 v[236:239], v112 offset:15360\n"
    "ds_read_b128 v[60:63], v112 offset:24576\n"
    "ds_read_b128 v[80:83], v112 offset:25600\n"
    "ds_read_b128 v[100:103], v112 offset:26624\n"
    "ds_read_b128 v[240:243], v112 offset:27648\n"
    "s_waitcnt lgkmcnt(15)\n"
    "v_min_f32 v244, v24, v26\n"
    "v_min_f32 v245, v28, v30\n"
    "v_min_f32 v246, v32, v34\n"
    "v_min_f32 v247, v36, v38\n"
    "v_min_f32 v248, v40, v42\n"
    "v_sub_f32 v24, v244, v24\n"
    "v_sub_f32 v28, v245, v28\n"
    "v_sub_f32 v32, v246, v32\n"
    "v_sub_f32 v36, v247, v36\n"
    "v_sub_f32 v40, v248, v40\n"
    "v_sub_f32 v26, v244, v26\n"
    "v_sub_f32 v30, v245, v30\n"
    "v_sub_f32 v34, v246, v34\n"
    "v_sub_f32 v38, v247, v38\n"
    "v_sub_f32 v42, v248, v42\n"
    "v_exp_f32 v24, v24\n"
    "v_exp_f32 v28, v28\n"
    "v_exp_f32 v32, v32\n"
    "v_exp_f32 v36, v36\n"
    "v_exp_f32 v40, v40\n"
    "v_exp_f32 v26, v26\n"
    "v_exp_f32 v30, v30\n"
    "v_exp_f32 v34, v34\n"
    "v_exp_f32 v38, v38\n"
    "v_exp_f32 v42, v42\n"
    "v_mul_f32 v244, v24, v25\n"
    "v_mul_f32 v245, v28, v29\n"
    "v_mul_f32 v246, v32, v33\n"
    "v_mul_f32 v247, v36, v37\n"
    "v_mul_f32 v248, v40, v41\n"
    "v_fmac_f32 v244, v26, v27\n"
    "v_fmac_f32 v245, v30, v31\n"
    "v_fmac_f32 v246, v34, v35\n"
    "v_fmac_f32 v247, v38, v39\n"
    "v_fmac_f32 v248, v42, v43\n"
    "v_rcp_f32 v244, v244\n"
    "v_rcp_f32 v245, v245\n"
    "v_rcp_f32 v246, v246\n"
    "v_rcp_f32 v247, v247\n"
    "v_rcp_f32 v248, v248\n"
    "v_mul_f32 v212, v24, v244\n"
    "v_mul_f32 v214, v28, v245\n"
    "v_mul_f32 v216, v32, v246\n"
    "v_mul_f32 v218, v36, v247\n"
    "v_mul_f32 v220, v40, v248\n"
    "v_mul_f32 v213, v26, v244\n"
    "v_mul_f32 v215, v30, v245\n"
    "v_mul_f32 v217, v34, v246\n"
    "v_mul_f32 v219, v38, v247\n"
    "v_mul_f32 v221, v42, v248\n"
    "s_waitcnt vmcnt(8) lgkmcnt(0)\n"
    "v_mul_f32 v44, v213, v44\n"
    "ds_read_b64_tr_b16 a[192:193], v13 offset:16384\n"
    "v_mul_f32 v45, v213, v45\n"
    "ds_read_b64_tr_b16 a[194:195], v13 offset:18432\n"
    "v_mul_f32 v46, v213, v46\n"
    "ds_read_b64_tr_b16 a[196:197], v14 offset:16384\n"
    "v_mul_f32 v47, v213, v47\n"
    "ds_read_b64_tr_b16 a[198:199], v14 offset:18432\n"
    "v_fmac_f32 v44, v212, v128\n"
    "ds_read_b64_tr_b16 a[200:201], v15 offset:16384\n"
    "v_fmac_f32 v45, v212, v129\n"
    "ds_read_b64_tr_b16 a[202:203], v15 offset:18432\n"
    "v_fmac_f32 v46, v212, v130\n"
    "ds_read_b64_tr_b16 a[204:205], v16 offset:16384\n"
    "v_fmac_f32 v47, v212, v131\n"
    "ds_read_b64_tr_b16 a[206:207], v16 offset:18432\n"
    "v_cvt_pk_bf16_f32 v44, v44, v45\n"
    "ds_read_b64_tr_b16 a[208:209], v13 offset:20480\n"
    "v_cvt_pk_bf16_f32 v45, v46, v47\n"
    "ds_read_b64_tr_b16 a[210:211], v13 offset:22528\n"
    "v_mul_f32 v64, v213, v64\n"
    "ds_read_b64_tr_b16 a[212:213], v14 offset:20480\n"
    "v_mul_f32 v65, v213, v65\n"
    "ds_read_b64_tr_b16 a[214:215], v14 offset:22528\n"
    "v_mul_f32 v66, v213, v66\n"
    "ds_read_b64_tr_b16 a[216:217], v15 offset:20480\n"
    "v_mul_f32 v67, v213, v67\n"
    "ds_read_b64_tr_b16 a[218:219], v15 offset:22528\n"
    "v_fmac_f32 v64, v212, v148\n"
    "ds_read_b64_tr_b16 a[220:221], v16 offset:20480\n"
    "v_fmac_f32 v65, v212, v149\n"
    "ds_read_b64_tr_b16 a[222:223], v16 offset:22528\n"
    "v_fmac_f32 v66, v212, v150\n"
    "v_fmac_f32 v67, v212, v151\n"
    "v_cvt_pk_bf16_f32 v46, v64, v65\n"
    "v_cvt_pk_bf16_f32 v47, v66, v67\n"
    "ds_write_b128 v112, v[44:47] offset:0\n"
    "v_mul_f32 v84, v213, v84\n"
    "v_mul_f32 v85, v213, v85\n"
    "v_mul_f32 v86, v213, v86\n"
    "v_mul_f32 v87, v213, v87\n"
    "v_fmac_f32 v84, v212, v168\n"
    "v_fmac_f32 v85, v212, v169\n"
    "v_fmac_f32 v86, v212, v170\n"
    "v_fmac_f32 v87, v212, v171\n"
    "v_cvt_pk_bf16_f32 v84, v84, v85\n"
    "v_cvt_pk_bf16_f32 v85, v86, v87\n"
    "v_mul_f32 v224, v213, v224\n"
    "v_mul_f32 v225, v213, v225\n"
    "v_mul_f32 v226, v213, v226\n"
    "v_mul_f32 v227, v213, v227\n"
    "v_fmac_f32 v224, v212, v188\n"
    "v_fmac_f32 v225, v212, v189\n"
    "v_fmac_f32 v226, v212, v190\n"
    "v_fmac_f32 v227, v212, v191\n"
    "v_cvt_pk_bf16_f32 v86, v224, v225\n"
    "v_cvt_pk_bf16_f32 v87, v226, v227\n"
    "ds_write_b128 v112, v[84:87] offset:5120\n"
    "v_mul_f32 v48, v215, v48\n"
    "v_mul_f32 v49, v215, v49\n"
    "v_mul_f32 v50, v215, v50\n"
    "v_mfma_f32_16x16x32_bf16 v[128:131], a[160:163], v[44:47], 0\n"
    "v_mul_f32 v51, v215, v51\n"
    "v_fmac_f32 v48, v214, v132\n"
    "v_fmac_f32 v49, v214, v133\n"
    "v_fmac_f32 v50, v214, v134\n"
    "v_fmac_f32 v51, v214, v135\n"
    "v_mfma_f32_16x16x32_bf16 v[148:151], a[164:167], v[44:47], 0\n"
    "v_cvt_pk_bf16_f32 v48, v48, v49\n"
    "v_cvt_pk_bf16_f32 v49, v50, v51\n"
    "v_mul_f32 v68, v215, v68\n"
    "v_mul_f32 v69, v215, v69\n"
    "v_mul_f32 v70, v215, v70\n"
    "v_mfma_f32_16x16x32_bf16 v[168:171], a[168:171], v[44:47], 0\n"
    "v_mul_f32 v71, v215, v71\n"
    "v_fmac_f32 v68, v214, v152\n"
    "v_fmac_f32 v69, v214, v153\n"
    "v_fmac_f32 v70, v214, v154\n"
    "v_fmac_f32 v71, v214, v155\n"
    "v_mfma_f32_16x16x32_bf16 v[188:191], a[172:175], v[44:47], 0\n"
    "v_cvt_pk_bf16_f32 v50, v68, v69\n"
    "v_cvt_pk_bf16_f32 v51, v70, v71\n"
    "ds_write_b128 v112, v[48:51] offset:1024\n"
    "v_mul_f32 v88, v215, v88\n"
    "v_mul_f32 v89, v215, v89\n"
    "v_mfma_f32_16x16x32_bf16 v[128:131], a[176:179], v[84:87], v[128:131]\n"
    "v_mul_f32 v90, v215, v90\n"
    "v_mul_f32 v91, v215, v91\n"
    "v_fmac_f32 v88, v214, v172\n"
    "v_fmac_f32 v89, v214, v173\n"
    "v_fmac_f32 v90, v214, v174\n"
    "v_mfma_f32_16x16x32_bf16 v[148:151], a[180:183], v[84:87], v[148:151]\n"
    "v_fmac_f32 v91, v214, v175\n"
    "v_cvt_pk_bf16_f32 v88, v88, v89\n"
    "v_cvt_pk_bf16_f32 v89, v90, v91\n"
    "v_mul_f32 v228, v215, v228\n"
    "v_mul_f32 v229, v215, v229\n"
    "v_mfma_f32_16x16x32_bf16 v[168:171], a[184:187], v[84:87], v[168:171]\n"
    "v_mul_f32 v230, v215, v230\n"
    "v_mul_f32 v231, v215, v231\n"
    "v_fmac_f32 v228, v214, v192\n"
    "v_fmac_f32 v229, v214, v193\n"
    "v_fmac_f32 v230, v214, v194\n"
    "v_mfma_f32_16x16x32_bf16 v[188:191], a[188:191], v[84:87], v[188:191]\n"
    "v_fmac_f32 v231, v214, v195\n"
    "v_cvt_pk_bf16_f32 v90, v228, v229\n"
    "v_cvt_pk_bf16_f32 v91, v230, v231\n"
    "ds_write_b128 v112, v[88:91] offset:6144\n"
    "v_mul_f32 v52, v217, v52\n"
    "s_waitcnt lgkmcnt(2)\n"
    "v_mul_f32 v53, v217, v53\n"
    "v_mul_f32 v54, v217, v54\n"
    "v_mfma_f32_16x16x32_bf16 v[132:135], a[160:163], v[48:51], 0\n"
    "v_mul_f32 v55, v217, v55\n"
    "v_fmac_f32 v52, v216, v136\n"
    "s_add_u32 s66, %2, 3\n"
    "s_and_b32 s66, s66, 3\n"
    "s_lshl_b32 s66, s66, 16\n"
    "s_lshr_b32 s67, s20, 2\n"
    "s_xor_b32 s67, s67, 0\n"
    "s_lshl_b32 s67, s67, 7\n"
    "s_add_u32 s66, s66, s67\n"
    "s_add_u32 s67, s66, 32768\n"
    "s_add_u32 m0, s56, 16384\n"
    "s_nop 0\n"
    "buffer_load_dwordx4 v222, s[44:47], s66 offen offset:0 lds\n"
    "v_fmac_f32 v53, v216, v137\n"
    "v_fmac_f32 v54, v216, v138\n"
    "v_fmac_f32 v55, v216, v139\n"
    "v_mfma_f32_16x16x32_bf16 v[152:155], a[164:167], v[48:51], 0\n"
    "v_cvt_pk_bf16_f32 v52, v52, v53\n"
    "v_cvt_pk_bf16_f32 v53, v54, v55\n"
    "v_mul_f32 v72, v217, v72\n"
    "v_mul_f32 v73, v217, v73\n"
    "v_mul_f32 v74, v217, v74\n"
    "v_mfma_f32_16x16x32_bf16 v[172:175], a[168:171], v[48:51], 0\n"
    "v_mul_f32 v75, v217, v75\n"
    "v_fmac_f32 v72, v216, v156\n"
    "v_fmac_f32 v73, v216, v157\n"
    "v_fmac_f32 v74, v216, v158\n"
    "v_fmac_f32 v75, v216, v159\n"
    "v_mfma_f32_16x16x32_bf16 v[192:195], a[172:175], v[48:51], 0\n"
    "v_cvt_pk_bf16_f32 v54, v72, v73\n"
    "buffer_load_dwordx4 v222, s[44:47], s66 offen offset:1024 lds\n"
    "v_cvt_pk_bf16_f32 v55, v74, v75\n"
    "ds_write_b128 v112, v[52:55] offset:2048\n"
    "v_mul_f32 v92, v217, v92\n"
    "v_mul_f32 v93, v217, v93\n"
    "v_mfma_f32_16x16x32_bf16 v[132:135], a[176:179], v[88:91], v[132:135]\n"
    "v_mul_f32 v94, v217, v94\n"
    "v_mul_f32 v95, v217, v95\n"
    "v_fmac_f32 v92, v216, v176\n"
    "v_fmac_f32 v93, v216, v177\n"
    "v_fmac_f32 v94, v216, v178\n"
    "v_mfma_f32_16x16x32_bf16 v[152:155], a[180:183], v[88:91], v[152:155]\n"
    "v_fmac_f32 v95, v216, v179\n"
    "v_cvt_pk_bf16_f32 v92, v92, v93\n"
    "v_cvt_pk_bf16_f32 v93, v94, v95\n"
    "v_mul_f32 v232, v217, v232\n"
    "v_mul_f32 v233, v217, v233\n"
    "v_mfma_f32_16x16x32_bf16 v[172:175], a[184:187], v[88:91], v[172:175]\n"
    "buffer_load_dwordx4 v223, s[44:47], s66 offen offset:2048 lds\n"
    "v_mul_f32 v234, v217, v234\n"
    "v_mul_f32 v235, v217, v235\n"
    "v_fmac_f32 v232, v216, v196\n"
    "v_fmac_f32 v233, v216, v197\n"
    "v_fmac_f32 v234, v216, v198\n"
    "v_mfma_f32_16x16x32_bf16 v[192:195], a[188:191], v[88:91], v[192:195]\n"
    "v_fmac_f32 v235, v216, v199\n"
    "v_cvt_pk_bf16_f32 v94, v232, v233\n"
    "v_cvt_pk_bf16_f32 v95, v234, v235\n"
    "ds_write_b128 v112, v[92:95] offset:7168\n"
    "v_mul_f32 v56, v219, v56\n"
    "v_mul_f32 v57, v219, v57\n"
    "v_mul_f32 v58, v219, v58\n"
    "v_mfma_f32_16x16x32_bf16 v[136:139], a[160:163], v[52:55], 0\n"
    "v_mul_f32 v59, v219, v59\n"
    "v_fmac_f32 v56, v218, v140\n"
    "buffer_load_dwordx4 v223, s[44:47], s66 offen offset:3072 lds\n"
    "v_fmac_f32 v57, v218, v141\n"
    "v_fmac_f32 v58, v218, v142\n"
    "v_fmac_f32 v59, v218, v143\n"
    "v_mfma_f32_16x16x32_bf16 v[156:159], a[164:167], v[52:55], 0\n"
    "v_cvt_pk_bf16_f32 v56, v56, v57\n"
    "v_cvt_pk_bf16_f32 v57, v58, v59\n"
    "v_mul_f32 v76, v219, v76\n"
    "v_mul_f32 v77, v219, v77\n"
    "v_mul_f32 v78, v219, v78\n"
    "v_mfma_f32_16x16x32_bf16 v[176:179], a[168:171], v[52:55], 0\n"
    "v_mul_f32 v79, v219, v79\n"
    "v_fmac_f32 v76, v218, v160\n"
    "v_fmac_f32 v77, v218, v161\n"
    "v_fmac_f32 v78, v218, v162\n"
    "v_fmac_f32 v79, v218, v163\n"
    "v_mfma_f32_16x16x32_bf16 v[196:199], a[172:175], v[52:55], 0\n"
    "v_cvt_pk_bf16_f32 v58, v76, v77\n"
    "s_add_u32 m0, s56, 20480\n"
    "s_nop 0\n"
    "buffer_load_dwordx4 v222, s[44:47], s67 offen offset:0 lds\n"
    "v_cvt_pk_bf16_f32 v59, v78, v79\n"
    "ds_write_b128 v112, v[56:59] offset:3072\n"
    "v_mul_f32 v96, v219, v96\n"
    "v_mul_f32 v97, v219, v97\n"
    "v_mfma_f32_16x16x32_bf16 v[136:139], a[176:179], v[92:95], v[136:139]\n"
    "v_mul_f32 v98, v219, v98\n"
    "v_mul_f32 v99, v219, v99\n"
    "v_fmac_f32 v96, v218, v180\n"
    "v_fmac_f32 v97, v218, v181\n"
    "v_fmac_f32 v98, v218, v182\n"
    "v_mfma_f32_16x16x32_bf16 v[156:159], a[180:183], v[92:95], v[156:159]\n"
    "v_fmac_f32 v99, v218, v183\n"
    "v_cvt_pk_bf16_f32 v96, v96, v97\n"
    "v_cvt_pk_bf16_f32 v97, v98, v99\n"
    "v_mul_f32 v236, v219, v236\n"
    "v_mul_f32 v237, v219, v237\n"
    "v_mfma_f32_16x16x32_bf16 v[176:179], a[184:187], v[92:95], v[176:179]\n"
    "buffer_load_dwordx4 v222, s[44:47], s67 offen offset:1024 lds\n"
    "v_mul_f32 v238, v219, v238\n"
    "v_mul_f32 v239, v219, v239\n"
    "v_fmac_f32 v236, v218, v200\n"
    "v_fmac_f32 v237, v218, v201\n"
    "v_fmac_f32 v238, v218, v202\n"
    "v_mfma_f32_16x16x32_bf16 v[196:199], a[188:191], v[92:95], v[196:199]\n"
    "v_fmac_f32 v239, v218, v203\n"
    "v_cvt_pk_bf16_f32 v98, v236, v237\n"
    "v_cvt_pk_bf16_f32 v99, v238, v239\n"
    "ds_write_b128 v112, v[96:99] offset:8192\n"
    "v_mul_f32 v60, v221, v60\n"
    "v_mul_f32 v61, v221, v61\n"
    "v_mul_f32 v62, v221, v62\n"
    "v_mfma_f32_16x16x32_bf16 v[140:143], a[160:163], v[56:59], 0\n"
    "v_mul_f32 v63, v221, v63\n"
    "v_fmac_f32 v60, v220, v144\n"
    "buffer_load_dwordx4 v223, s[44:47], s67 offen offset:2048 lds\n"
    "v_fmac_f32 v61, v220, v145\n"
    "v_fmac_f32 v62, v220, v146\n"
    "v_fmac_f32 v63, v220, v147\n"
    "v_mfma_f32_16x16x32_bf16 v[160:163], a[164:167], v[56:59], 0\n"
    "v_cvt_pk_bf16_f32 v60, v60, v61\n"
    "v_cvt_pk_bf16_f32 v61, v62, v63\n"
    "v_mul_f32 v80, v221, v80\n"
    "v_mul_f32 v81, v221, v81\n"
    "v_mul_f32 v82, v221, v82\n"
    "v_mfma_f32_16x16x32_bf16 v[180:183], a[168:171], v[56:59], 0\n"
    "v_mul_f32 v83, v221, v83\n"
    "v_fmac_f32 v80, v220, v164\n"
    "v_fmac_f32 v81, v220, v165\n"
    "v_fmac_f32 v82, v220, v166\n"
    "v_fmac_f32 v83, v220, v167\n"
    "v_mfma_f32_16x16x32_bf16 v[200:203], a[172:175], v[56:59], 0\n"
    "v_cvt_pk_bf16_f32 v62, v80, v81\n"
    "buffer_load_dwordx4 v223, s[44:47], s67 offen offset:3072 lds\n"
    "v_cvt_pk_bf16_f32 v63, v82, v83\n"
    "ds_write_b128 v112, v[60:63] offset:4096\n"
    "v_mul_f32 v100, v221, v100\n"
    "v_mul_f32 v101, v221, v101\n"
    "v_mfma_f32_16x16x32_bf16 v[140:143], a[176:179], v[96:99], v[140:143]\n"
    "v_mul_f32 v102, v221, v102\n"
    "v_mul_f32 v103, v221, v103\n"
    "v_fmac_f32 v100, v220, v184\n"
    "v_fmac_f32 v101, v220, v185\n"
    "v_fmac_f32 v102, v220, v186\n"
    "v_mfma_f32_16x16x32_bf16 v[160:163], a[180:183], v[96:99], v[160:163]\n"
    "v_fmac_f32 v103, v220, v187\n"
    "v_cvt_pk_bf16_f32 v100, v100, v101\n"
    "v_cvt_pk_bf16_f32 v101, v102, v103\n"
    "v_mul_f32 v240, v221, v240\n"
    "v_mul_f32 v241, v221, v241\n"
    "v_mfma_f32_16x16x32_bf16 v[180:183], a[184:187], v[96:99], v[180:183]\n"
    "v_mul_f32 v242, v221, v242\n"
    "v_mul_f32 v243, v221, v243\n"
    "v_fmac_f32 v240, v220, v204\n"
    "v_fmac_f32 v241, v220, v205\n"
    "v_fmac_f32 v242, v220, v206\n"
    "v_mfma_f32_16x16x32_bf16 v[200:203], a[188:191], v[96:99], v[200:203]\n"
    "v_fmac_f32 v243, v220, v207\n"
    "v_cvt_pk_bf16_f32 v102, v240, v241\n"
    "v_cvt_pk_bf16_f32 v103, v242, v243\n"
    "ds_write_b128 v112, v[100:103] offset:9216\n"
    "s_waitcnt lgkmcnt(0)\n"
    "s_barrier\n"
    "ds_read_b128 a[40:43], v119 offset:0\n"
    "ds_read_b128 a[44:47], v119 offset:1024\n"
    "ds_read_b128 a[48:51], v119 offset:2048\n"
    "ds_read_b128 a[52:55], v119 offset:3072\n"
    "ds_read_b128 a[56:59], v119 offset:4096\n"
    "ds_read_b128 a[60:63], v119 offset:5120\n"
    "ds_read_b128 a[64:67], v119 offset:6144\n"
    "ds_read_b128 a[68:71], v119 offset:7168\n"
    "ds_read_b128 a[72:75], v119 offset:8192\n"
    "ds_read_b128 a[76:79], v119 offset:9216\n"
    "ds_read_b128 a[80:83], v120 offset:0\n"
    "ds_read_b128 a[84:87], v120 offset:1024\n"
    "ds_read_b128 a[88:91], v120 offset:2048\n"
    "ds_read_b128 a[92:95], v120 offset:3072\n"
    "v_mfma_f32_16x16x32_bf16 v[144:147], a[160:163], v[60:63], 0\n"
    "v_mfma_f32_16x16x32_bf16 v[164:167], a[164:167], v[60:63], 0\n"
    "v_mfma_f32_16x16x32_bf16 v[184:187], a[168:171], v[60:63], 0\n"
    "v_mfma_f32_16x16x32_bf16 v[204:207], a[172:175], v[60:63], 0\n"
    "v_mfma_f32_16x16x32_bf16 v[144:147], a[176:179], v[100:103], v[144:147]\n"
    "v_mfma_f32_16x16x32_bf16 v[164:167], a[180:183], v[100:103], v[164:167]\n"
    "v_mfma_f32_16x16x32_bf16 v[184:187], a[184:187], v[100:103], v[184:187]\n"
    "v_mfma_f32_16x16x32_bf16 v[204:207], a[188:191], v[100:103], v[204:207]\n"
    "s_waitcnt vmcnt(8) lgkmcnt(4)\n"
    "v_mfma_f32_16x16x32_bf16 v[128:131], a[192:195], a[40:43], v[128:131]\n"
    "ds_read_b128 a[96:99], v120 offset:4096\n"
    "s_add_u32 s66, %2, 0\n"
    "v_mfma_f32_16x16x32_bf16 v[132:135], a[192:195], a[44:47], v[132:135]\n"
    "ds_read_b128 a[100:103], v120 offset:5120\n"
    "s_and_b32 s66, s66, 3\n"
    "v_mfma_f32_16x16x32_bf16 v[136:139], a[192:195], a[48:51], v[136:139]\n"
    "ds_read_b128 a[104:107], v120 offset:6144\n"
    "s_lshl_b32 s66, s66, 16\n"
    "v_mfma_f32_16x16x32_bf16 v[140:143], a[192:195], a[52:55], v[140:143]\n"
    "ds_read_b128 a[108:111], v120 offset:7168\n"
    "s_lshr_b32 s67, s20, 2\n"
    "s_xor_b32 s67, s67, 1\n"
    "s_lshl_b32 s67, s67, 7\n"
    "s_add_u32 s66, s66, s67\n"
    "s_add_u32 s67, s66, 32768\n"
    "s_add_u32 m0, s56, 24576\n"
    "v_mfma_f32_16x16x32_bf16 v[144:147], a[192:195], a[56:59], v[144:147]\n"
    "ds_read_b128 a[112:115], v120 offset:8192\n"
    "buffer_load_dwordx4 v222, s[44:47], s66 offen offset:0 lds\n"
    "v_mfma_f32_16x16x32_bf16 v[148:151], a[196:199], a[40:43], v[148:151]\n"
    "ds_read_b128 a[116:119], v120 offset:9216\n"
    "v_mfma_f32_16x16x32_bf16 v[152:155], a[196:199], a[44:47], v[152:155]\n"
    "buffer_load_dwordx4 v222, s[44:47], s66 offen offset:1024 lds\n"
    "v_mfma_f32_16x16x32_bf16 v[156:159], a[196:199], a[48:51], v[156:159]\n"
    "ds_read_b128 a[120:123], v121 offset:0\n"
    "v_mfma_f32_16x16x32_bf16 v[160:163], a[196:199], a[52:55], v[160:163]\n"
    "ds_read_b128 a[124:127], v121 offset:1024\n"
    "v_mfma_f32_16x16x32_bf16 v[164:167], a[196:199], a[56:59], v[164:167]\n"
    "ds_read_b128 a[128:131], v121 offset:2048\n"
    "v_mfma_f32_16x16x32_bf16 v[168:171], a[200:203], a[40:43], v[168:171]\n"
    "ds_read_b128 a[132:135], v121 offset:3072\n"
    "v_mfma_f32_16x16x32_bf16 v[172:175], a[200:203], a[44:47], v[172:175]\n"
    "ds_read_b128 a[136:139], v121 offset:4096\n"
    "v_mfma_f32_16x16x32_bf16 v[176:179], a[200:203], a[48:51], v[176:179]\n"
    "buffer_load_dwordx4 v223, s[44:47], s66 offen offset:2048 lds\n"
    "v_mfma_f32_16x16x32_bf16 v[180:183], a[200:203], a[52:55], v[180:183]\n"
    "ds_read_b128 a[140:143], v121 offset:5120\n"
    "v_mfma_f32_16x16x32_bf16 v[184:187], a[200:203], a[56:59], v[184:187]\n"
    "ds_read_b128 a[144:147], v121 offset:6144\n"
    "v_mfma_f32_16x16x32_bf16 v[188:191], a[204:207], a[40:43], v[188:191]\n"
    "ds_read_b128 a[148:151], v121 offset:7168\n"
    "v_mfma_f32_16x16x32_bf16 v[192:195], a[204:207], a[44:47], v[192:195]\n"
    "ds_read_b128 a[152:155], v121 offset:8192\n"
    "v_mfma_f32_16x16x32_bf16 v[196:199], a[204:207], a[48:51], v[196:199]\n"
    "ds_read_b128 a[156:159], v121 offset:9216\n"
    "v_mfma_f32_16x16x32_bf16 v[200:203], a[204:207], a[52:55], v[200:203]\n"
    "buffer_load_dwordx4 v223, s[44:47], s66 offen offset:3072 lds\n"
    "s_add_u32 m0, s56, 28672\n"
    "v_mfma_f32_16x16x32_bf16 v[204:207], a[204:207], a[56:59], v[204:207]\n"
    "ds_read_b128 a[0:3], v118 offset:0\n"
    "v_mfma_f32_16x16x32_bf16 v[128:131], a[208:211], a[60:63], v[128:131]\n"
    "ds_read_b128 a[4:7], v118 offset:1024\n"
    "v_mfma_f32_16x16x32_bf16 v[132:135], a[208:211], a[64:67], v[132:135]\n"
    "ds_read_b128 a[8:11], v118 offset:2048\n"
    "v_mfma_f32_16x16x32_bf16 v[136:139], a[208:211], a[68:71], v[136:139]\n"
    "ds_read_b128 a[12:15], v118 offset:3072\n"
    "v_mfma_f32_16x16x32_bf16 v[140:143], a[208:211], a[72:75], v[140:143]\n"
    "ds_read_b128 a[16:19], v118 offset:4096\n"
    "v_mfma_f32_16x16x32_bf16 v[144:147], a[208:211], a[76:79], v[144:147]\n"
    "buffer_load_dwordx4 v222, s[44:47], s67 offen offset:0 lds\n"
    "v_mfma_f32_16x16x32_bf16 v[148:151], a[212:215], a[60:63], v[148:151]\n"
    "ds_read_b128 a[20:23], v118 offset:5120\n"
    "v_mfma_f32_16x16x32_bf16 v[152:155], a[212:215], a[64:67], v[152:155]\n"
    "ds_read_b128 a[24:27], v118 offset:6144\n"
    "v_mfma_f32_16x16x32_bf16 v[156:159], a[212:215], a[68:71], v[156:159]\n"
    "ds_read_b128 a[28:31], v118 offset:7168\n"
    "v_mfma_f32_16x16x32_bf16 v[160:163], a[212:215], a[72:75], v[160:163]\n"
    "ds_read_b128 a[32:35], v118 offset:8192\n"
    "v_mfma_f32_16x16x32_bf16 v[164:167], a[212:215], a[76:79], v[164:167]\n"
    "ds_read_b128 a[36:39], v118 offset:9216\n"
    "ds_read_b64_tr_b16 a[160:161], v13 offset:32768\n"
    "ds_read_b64_tr_b16 a[162:163], v13 offset:34816\n"
    "ds_read_b64_tr_b16 a[164:165], v14 offset:32768\n"
    "ds_read_b64_tr_b16 a[166:167], v14 offset:34816\n"
    "ds_read_b64_tr_b16 a[168:169], v15 offset:32768\n"
    "ds_read_b64_tr_b16 a[170:171], v15 offset:34816\n"
    "ds_read_b64_tr_b16 a[172:173], v16 offset:32768\n"
    "ds_read_b64_tr_b16 a[174:175], v16 offset:34816\n"
    "ds_read_b64_tr_b16 a[176:177], v13 offset:36864\n"
    "ds_read_b64_tr_b16 a[178:179], v13 offset:38912\n"
    "ds_read_b64_tr_b16 a[180:181], v14 offset:36864\n"
    "buffer_load_dwordx4 v222, s[44:47], s67 offen offset:1024 lds\n"
    "v_mfma_f32_16x16x32_bf16 v[168:171], a[216:219], a[60:63], v[168:171]\n"
    "ds_read_b64_tr_b16 a[182:183], v14 offset:38912\n"
    "v_mfma_f32_16x16x32_bf16 v[172:175], a[216:219], a[64:67], v[172:175]\n"
    "ds_read_b64_tr_b16 a[184:185], v15 offset:36864\n"
    "v_mfma_f32_16x16x32_bf16 v[176:179], a[216:219], a[68:71], v[176:179]\n"
    "ds_read_b64_tr_b16 a[186:187], v15 offset:38912\n"
    "v_mfma_f32_16x16x32_bf16 v[180:183], a[216:219], a[72:75], v[180:183]\n"
    "buffer_load_dwordx4 v223, s[44:47], s67 offen offset:2048 lds\n"
    "v_mfma_f32_16x16x32_bf16 v[184:187], a[216:219], a[76:79], v[184:187]\n"
    "ds_read_b64_tr_b16 a[188:189], v16 offset:36864\n"
    "v_mfma_f32_16x16x32_bf16 v[188:191], a[220:223], a[60:63], v[188:191]\n"
    "ds_read_b64_tr_b16 a[190:191], v16 offset:38912\n"
    "v_mfma_f32_16x16x32_bf16 v[192:195], a[220:223], a[64:67], v[192:195]\n"
    "buffer_load_dwordx4 v223, s[44:47], s67 offen offset:3072 lds\n"
    "v_mfma_f32_16x16x32_bf16 v[196:199], a[220:223], a[68:71], v[196:199]\n"
    "v_mfma_f32_16x16x32_bf16 v[200:203], a[220:223], a[72:75], v[200:203]\n"
    "v_mfma_f32_16x16x32_bf16 v[204:207], a[220:223], a[76:79], v[204:207]\n"
    "s_waitcnt vmcnt(8) lgkmcnt(0)\n"
    "s_barrier\n"
    "v_mfma_f32_16x16x32_bf16 v[128:131], a[160:163], a[80:83], v[128:131]\n"
    "ds_read_b64_tr_b16 a[192:193], v13 offset:16384\n"
    "s_add_u32 s66, %2, 1\n"
    "v_mfma_f32_16x16x32_bf16 v[132:135], a[160:163], a[84:87], v[132:135]\n"
    "s_and_b32 s66, s66, 3\n"
    "s_lshl_b32 s66, s66, 16\n"
    "s_lshr_b32 s67, s20, 2\n"
    "v_mfma_f32_16x16x32_bf16 v[136:139], a[160:163], a[88:91], v[136:139]\n"
    "ds_read_b64_tr_b16 a[194:195], v13 offset:18432\n"
    "s_xor_b32 s67, s67, 1\n"
    "v_mfma_f32_16x16x32_bf16 v[140:143], a[160:163], a[92:95], v[140:143]\n"
    "s_lshl_b32 s67, s67, 7\n"
    "s_add_u32 s66, s66, s67\n"
    "s_add_u32 s67, s66, 32768\n"
    "s_add_u32 m0, s56, 32768\n"
    "v_mfma_f32_16x16x32_bf16 v[144:147], a[160:163], a[96:99], v[144:147]\n"
    "buffer_load_dwordx4 v222, s[44:47], s66 offen offset:0 lds\n"
    "v_mfma_f32_16x16x32_bf16 v[148:151], a[164:167], a[80:83], v[148:151]\n"
    "buffer_load_dwordx4 v222, s[44:47], s66 offen offset:1024 lds\n"
    "v_mfma_f32_16x16x32_bf16 v[152:155], a[164:167], a[84:87], v[152:155]\n"
    "ds_read_b64_tr_b16 a[196:197], v14 offset:16384\n"
    "v_mfma_f32_16x16x32_bf16 v[156:159], a[164:167], a[88:91], v[156:159]\n"
    "buffer_load_dwordx4 v223, s[44:47], s66 offen offset:2048 lds\n"
    "v_mfma_f32_16x16x32_bf16 v[160:163], a[164:167], a[92:95], v[160:163]\n"
    "ds_read_b64_tr_b16 a[198:199], v14 offset:18432\n"
    "v_mfma_f32_16x16x32_bf16 v[164:167], a[164:167], a[96:99], v[164:167]\n"
    "ds_read_b64_tr_b16 a[200:201], v15 offset:16384\n"
    "v_mfma_f32_16x16x32_bf16 v[168:171], a[168:171], a[80:83], v[168:171]\n"
    "buffer_load_dwordx4 v223, s[44:47], s66 offen offset:3072 lds\n"
    "s_add_u32 m0, s56, 36864\n"
    "v_mfma_f32_16x16x32_bf16 v[172:175], a[168:171], a[84:87], v[172:175]\n"
    "ds_read_b64_tr_b16 a[202:203], v15 offset:18432\n"
    "s_add_u32 s66, %2, 2\n"
    "v_mfma_f32_16x16x32_bf16 v[176:179], a[168:171], a[88:91], v[176:179]\n"
    "ds_read_b64_tr_b16 a[204:205], v16 offset:16384\n"
    "s_and_b32 s66, s66, 3\n"
    "v_mfma_f32_16x16x32_bf16 v[180:183], a[168:171], a[92:95], v[180:183]\n"
    "buffer_load_dwordx4 v222, s[44:47], s67 offen offset:0 lds\n"
    "s_lshl_b32 s66, s66, 16\n"
    "v_mfma_f32_16x16x32_bf16 v[184:187], a[168:171], a[96:99], v[184:187]\n"
    "ds_read_b64_tr_b16 a[206:207], v16 offset:18432\n"
    "v_mfma_f32_16x16x32_bf16 v[188:191], a[172:175], a[80:83], v[188:191]\n"
    "v_mfma_f32_16x16x32_bf16 v[192:195], a[172:175], a[84:87], v[192:195]\n"
    "buffer_load_dwordx4 v222, s[44:47], s67 offen offset:1024 lds\n"
    "v_mfma_f32_16x16x32_bf16 v[196:199], a[172:175], a[88:91], v[196:199]\n"
    "ds_read_b64_tr_b16 a[208:209], v13 offset:20480\n"
    "v_mfma_f32_16x16x32_bf16 v[200:203], a[172:175], a[92:95], v[200:203]\n"
    "v_mfma_f32_16x16x32_bf16 v[204:207], a[172:175], a[96:99], v[204:207]\n"
    "ds_read_b64_tr_b16 a[210:211], v13 offset:22528\n"
    "v_mfma_f32_16x16x32_bf16 v[128:131], a[176:179], a[100:103], v[128:131]\n"
    "buffer_load_dwordx4 v223, s[44:47], s67 offen offset:2048 lds\n"
    "v_mfma_f32_16x16x32_bf16 v[132:135], a[176:179], a[104:107], v[132:135]\n"
    "ds_read_b64_tr_b16 a[212:213], v14 offset:20480\n"
    "v_mfma_f32_16x16x32_bf16 v[136:139], a[176:179], a[108:111], v[136:139]\n"
    "v_mfma_f32_16x16x32_bf16 v[140:143], a[176:179], a[112:115], v[140:143]\n"
    "buffer_load_dwordx4 v223, s[44:47], s67 offen offset:3072 lds\n"
    "s_lshr_b32 s67, s20, 2\n"
    "v_mfma_f32_16x16x32_bf16 v[144:147], a[176:179], a[116:119], v[144:147]\n"
    "ds_read_b64_tr_b16 a[214:215], v14 offset:22528\n"
    "s_xor_b32 s67, s67, 1\n"
    "v_mfma_f32_16x16x32_bf16 v[148:151], a[180:183], a[100:103], v[148:151]\n"
    "ds_read_b64_tr_b16 a[216:217], v15 offset:20480\n"
    "s_lshl_b32 s67, s67, 7\n"
    "v_mfma_f32_16x16x32_bf16 v[152:155], a[180:183], a[104:107], v[152:155]\n"
    "s_add_u32 s66, s66, s67\n"
    "s_add_u32 s67, s66, 32768\n"
    "s_add_u32 m0, s56, 0\n"
    "v_mfma_f32_16x16x32_bf16 v[156:159], a[180:183], a[108:111], v[156:159]\n"
    "buffer_load_dwordx4 v222, s[44:47], s66 offen offset:0 lds\n"
    "v_mfma_f32_16x16x32_bf16 v[160:163], a[180:183], a[112:115], v[160:163]\n"
    "ds_read_b64_tr_b16 a[218:219], v15 offset:22528\n"
    "v_mfma_f32_16x16x32_bf16 v[164:167], a[180:183], a[116:119], v[164:167]\n"
    "ds_read_b64_tr_b16 a[220:221], v16 offset:20480\n"
    "v_mfma_f32_16x16x32_bf16 v[168:171], a[184:187], a[100:103], v[168:171]\n"
    "buffer_load_dwordx4 v222, s[44:47], s66 offen offset:1024 lds\n"
    "v_mfma_f32_16x16x32_bf16 v[172:175], a[184:187], a[104:107], v[172:175]\n"
    "ds_read_b64_tr_b16 a[222:223], v16 offset:22528\n"
    "v_mfma_f32_16x16x32_bf16 v[176:179], a[184:187], a[108:111], v[176:179]\n"
    "v_mfma_f32_16x16x32_bf16 v[180:183], a[184:187], a[112:115], v[180:183]\n"
    "buffer_load_dwordx4 v223, s[44:47], s66 offen offset:2048 lds\n"
    "v_mfma_f32_16x16x32_bf16 v[184:187], a[184:187], a[116:119], v[184:187]\n"
    "v_mfma_f32_16x16x32_bf16 v[188:191], a[188:191], a[100:103], v[188:191]\n"
    "v_mfma_f32_16x16x32_bf16 v[192:195], a[188:191], a[104:107], v[192:195]\n"
    "buffer_load_dwordx4 v223, s[44:47], s66 offen offset:3072 lds\n"
    "v_mfma_f32_16x16x32_bf16 v[196:199], a[188:191], a[108:111], v[196:199]\n"
    "v_mfma_f32_16x16x32_bf16 v[200:203], a[188:191], a[112:115], v[200:203]\n"
    "v_mfma_f32_16x16x32_bf16 v[204:207], a[188:191], a[116:119], v[204:207]\n"
    "s_waitcnt vmcnt(12) lgkmcnt(0)\n"
    "v_mfma_f32_16x16x32_bf16 v[128:131], a[192:195], a[120:123], v[128:131]\n"
    "ds_read_b64_tr_b16 a[160:161], v13 offset:24576\n"
    "s_add_u32 m0, s56, 4096\n"
    "v_mfma_f32_16x16x32_bf16 v[132:135], a[192:195], a[124:127], v[132:135]\n"
    "buffer_load_dwordx4 v222, s[44:47], s67 offen offset:0 lds\n"
    "s_lshl_b32 s64, s20, 2\n"
    "v_mfma_f32_16x16x32_bf16 v[136:139], a[192:195], a[128:131], v[136:139]\n"
    "ds_read_b64_tr_b16 a[162:163], v13 offset:26624\n"
    "s_add_u32 s64, s64, %2\n"
    "v_mfma_f32_16x16x32_bf16 v[140:143], a[192:195], a[132:135], v[140:143]\n"
    "buffer_load_dwordx4 v222, s[44:47], s67 offen offset:1024 lds\n"
    "s_lshl_b32 s64, s64, 9\n"
    "v_mfma_f32_16x16x32_bf16 v[144:147], a[192:195], a[136:139], v[144:147]\n"
    "ds_read_b64_tr_b16 a[164:165], v14 offset:24576\n"
    "s_and_b32 s65, s69, 31\n"
    "v_mfma_f32_16x16x32_bf16 v[148:151], a[196:199], a[120:123], v[148:151]\n"
    "s_lshl_b32 s65, s65, 3\n"
    "s_add_u32 s64, s64, s65\n"
    "s_add_u32 s66, %2, 3\n"
    "v_mfma_f32_16x16x32_bf16 v[152:155], a[196:199], a[124:127], v[152:155]\n"
    "buffer_load_dwordx4 v223, s[44:47], s67 offen offset:2048 lds\n"
    "s_and_b32 s66, s66, 3\n"
    "v_mfma_f32_16x16x32_bf16 v[156:159], a[196:199], a[128:131], v[156:159]\n"
    "ds_read_b64_tr_b16 a[166:167], v14 offset:26624\n"
    "s_lshl_b32 s66, s66, 16\n"
    "v_mfma_f32_16x16x32_bf16 v[160:163], a[196:199], a[132:135], v[160:163]\n"
    "ds_read_b64_tr_b16 a[168:169], v15 offset:24576\n"
    "s_add_u32 s64, s64, 67174400\n"
    "v_mfma_f32_16x16x32_bf16 v[164:167], a[196:199], a[136:139], v[164:167]\n"
    "s_add_u32 s36, s14, s64\n"
    "s_addc_u32 s37, s15, 0\n"
    "s_and_b32 s37, s37, 0xffff\n"
    "v_mfma_f32_16x16x32_bf16 v[168:171], a[200:203], a[120:123], v[168:171]\n"
    "buffer_load_dwordx4 v223, s[44:47], s67 offen offset:3072 lds\n"
    "s_lshr_b32 s67, s20, 2\n"
    "v_mfma_f32_16x16x32_bf16 v[172:175], a[200:203], a[124:127], v[172:175]\n"
    "ds_read_b64_tr_b16 a[170:171], v15 offset:26624\n"
    "s_xor_b32 s67, s67, 1\n"
    "v_mfma_f32_16x16x32_bf16 v[176:179], a[200:203], a[128:131], v[176:179]\n"
    "ds_read_b64_tr_b16 a[172:173], v16 offset:24576\n"
    "s_lshl_b32 s67, s67, 7\n"
    "v_mfma_f32_16x16x32_bf16 v[180:183], a[200:203], a[132:135], v[180:183]\n"
    "s_add_u32 s66, s66, s67\n"
    "s_add_u32 s67, s66, 32768\n"
    "s_add_u32 m0, s56, 8192\n"
    "v_mfma_f32_16x16x32_bf16 v[184:187], a[200:203], a[136:139], v[184:187]\n"
    "buffer_load_dwordx4 v222, s[44:47], s66 offen offset:0 lds\n"
    "s_mov_b32 s38, 264\n"
    "v_mfma_f32_16x16x32_bf16 v[188:191], a[204:207], a[120:123], v[188:191]\n"
    "ds_read_b64_tr_b16 a[174:175], v16 offset:26624\n"
    "s_mov_b32 s39, 0x20000\n"
    "v_mfma_f32_16x16x32_bf16 v[192:195], a[204:207], a[124:127], v[192:195]\n"
    "buffer_load_dwordx4 v222, s[44:47], s66 offen offset:1024 lds\n"
    "s_lshr_b32 s58, s20, 2\n"
    "v_mfma_f32_16x16x32_bf16 v[196:199], a[204:207], a[128:131], v[196:199]\n"
    "ds_read_b64_tr_b16 a[176:177], v13 offset:28672\n"
    "s_lshl_b32 s58, s58, 8\n"
    "v_mfma_f32_16x16x32_bf16 v[200:203], a[204:207], a[132:135], v[200:203]\n"
    "s_xor_b32 s59, s58, 256\n"
    "v_mfma_f32_16x16x32_bf16 v[204:207], a[204:207], a[136:139], v[204:207]\n"
    "ds_read_b64_tr_b16 a[178:179], v13 offset:30720\n"
    "v_mfma_f32_16x16x32_bf16 v[128:131], a[208:211], a[140:143], v[128:131]\n"
    "buffer_load_dwordx4 v223, s[44:47], s66 offen offset:2048 lds\n"
    "v_mfma_f32_16x16x32_bf16 v[132:135], a[208:211], a[144:147], v[132:135]\n"
    "ds_read_b64_tr_b16 a[180:181], v14 offset:28672\n"
    "v_mfma_f32_16x16x32_bf16 v[136:139], a[208:211], a[148:151], v[136:139]\n"
    "v_mfma_f32_16x16x32_bf16 v[140:143], a[208:211], a[152:155], v[140:143]\n"
    "buffer_load_dwordx4 v223, s[44:47], s66 offen offset:3072 lds\n"
    "s_add_u32 m0, s56, 12288\n"
    "v_mfma_f32_16x16x32_bf16 v[144:147], a[208:211], a[156:159], v[144:147]\n"
    "ds_read_b64_tr_b16 a[182:183], v14 offset:30720\n"
    "v_mfma_f32_16x16x32_bf16 v[148:151], a[212:215], a[140:143], v[148:151]\n"
    "ds_read_b64_tr_b16 a[184:185], v15 offset:28672\n"
    "v_mfma_f32_16x16x32_bf16 v[152:155], a[212:215], a[144:147], v[152:155]\n"
    "buffer_load_dwordx4 v222, s[44:47], s67 offen offset:0 lds\n"
    "v_mfma_f32_16x16x32_bf16 v[156:159], a[212:215], a[148:151], v[156:159]\n"
    "ds_read_b64_tr_b16 a[186:187], v15 offset:30720\n"
    "v_mfma_f32_16x16x32_bf16 v[160:163], a[212:215], a[152:155], v[160:163]\n"
    "v_mfma_f32_16x16x32_bf16 v[164:167], a[212:215], a[156:159], v[164:167]\n"
    "ds_read_b64_tr_b16 a[188:189], v16 offset:28672\n"
    "v_mfma_f32_16x16x32_bf16 v[168:171], a[216:219], a[140:143], v[168:171]\n"
    "buffer_load_dwordx4 v222, s[44:47], s67 offen offset:1024 lds\n"
    "v_mfma_f32_16x16x32_bf16 v[172:175], a[216:219], a[144:147], v[172:175]\n"
    "ds_read_b64_tr_b16 a[190:191], v16 offset:30720\n"
    "v_mfma_f32_16x16x32_bf16 v[176:179], a[216:219], a[148:151], v[176:179]\n"
    "v_mov_b32 v10, s28\n"
    "v_mfma_f32_16x16x32_bf16 v[180:183], a[216:219], a[152:155], v[180:183]\n"
    "buffer_load_dwordx4 v223, s[44:47], s67 offen offset:2048 lds\n"
    "v_mfma_f32_16x16x32_bf16 v[184:187], a[216:219], a[156:159], v[184:187]\n"
    "v_mfma_f32_16x16x32_bf16 v[188:191], a[220:223], a[140:143], v[188:191]\n"
    "v_mov_b32 v11, s29\n"
    "v_mfma_f32_16x16x32_bf16 v[192:195], a[220:223], a[144:147], v[192:195]\n"
    "buffer_load_dwordx4 v223, s[44:47], s67 offen offset:3072 lds\n"
    "v_mfma_f32_16x16x32_bf16 v[196:199], a[220:223], a[148:151], v[196:199]\n"
    "v_lshlrev_b32 v9, 10, v0\n"
    "v_mfma_f32_16x16x32_bf16 v[200:203], a[220:223], a[152:155], v[200:203]\n"
    "v_mfma_f32_16x16x32_bf16 v[204:207], a[220:223], a[156:159], v[204:207]\n"
    "s_waitcnt vmcnt(16) lgkmcnt(0)\n"
    "v_mfma_f32_16x16x32_bf16 v[24:27], a[160:163], a[0:3], 0\n"
    "ds_read_b64_tr_b16 a[192:193], v13 offset:32768\n"
    "s_lshr_b32 s71, s20, 2\n"
    "v_mfma_f32_16x16x32_bf16 v[28:31], a[160:163], a[4:7], 0\n"
    "s_xor_b32 s71, s71, 0\n"
    "s_mul_i32 s71, s71, 10240\n"
    "s_add_u32 s60, s71, 0\n"
    "v_mfma_f32_16x16x32_bf16 v[32:35], a[160:163], a[8:11], 0\n"
    "ds_read_b64_tr_b16 a[194:195], v13 offset:34816\n"
    "s_add_u32 s61, s71, 1024\n"
    "v_mfma_f32_16x16x32_bf16 v[36:39], a[160:163], a[12:15], 0\n"
    "v_mfma_f32_16x16x32_bf16 v[40:43], a[160:163], a[16:19], 0\n"
    "ds_read_b64_tr_b16 a[196:197], v14 offset:32768\n"
    "v_mfma_f32_16x16x32_bf16 v[44:47], a[164:167], a[0:3], 0\n"
    "v_mfma_f32_16x16x32_bf16 v[48:51], a[164:167], a[4:7], 0\n"
    "ds_read_b64_tr_b16 a[198:199], v14 offset:34816\n"
    "v_cvt_pk_bf16_f32 v224, v128, v129\n"
    "v_mfma_f32_16x16x32_bf16 v[52:55], a[164:167], a[8:11], 0\n"
    "v_cvt_pk_bf16_f32 v225, v130, v131\n"
    "v_mfma_f32_16x16x32_bf16 v[56:59], a[164:167], a[12:15], 0\n"
    "ds_read_b64_tr_b16 a[200:201], v15 offset:32768\n"
    "v_cvt_pk_bf16_f32 v226, v148, v149\n"
    "v_mfma_f32_16x16x32_bf16 v[60:63], a[164:167], a[16:19], 0\n"
    "v_cvt_pk_bf16_f32 v227, v150, v151\n"
    "v_mfma_f32_16x16x32_bf16 v[64:67], a[168:171], a[0:3], 0\n"
    "ds_read_b64_tr_b16 a[202:203], v15 offset:34816\n"
    "v_mfma_f32_16x16x32_bf16 v[68:71], a[168:171], a[4:7], 0\n"
    "buffer_store_dwordx4 v[224:227], v123, s[52:55], s60 offen sc1\n"
    "s_add_u32 s60, s71, 2048\n"
    "v_mfma_f32_16x16x32_bf16 v[72:75], a[168:171], a[8:11], 0\n"
    "ds_read_b64_tr_b16 a[204:205], v16 offset:32768\n"
    "v_mfma_f32_16x16x32_bf16 v[76:79], a[168:171], a[12:15], 0\n"
    "v_cvt_pk_bf16_f32 v228, v132, v133\n"
    "v_mfma_f32_16x16x32_bf16 v[80:83], a[168:171], a[16:19], 0\n"
    "ds_read_b64_tr_b16 a[206:207], v16 offset:34816\n"
    "v_cvt_pk_bf16_f32 v229, v134, v135\n"
    "v_mfma_f32_16x16x32_bf16 v[84:87], a[172:175], a[0:3], 0\n"
    "v_cvt_pk_bf16_f32 v230, v152, v153\n"
    "v_mfma_f32_16x16x32_bf16 v[88:91], a[172:175], a[4:7], 0\n"
    "v_cvt_pk_bf16_f32 v231, v154, v155\n"
    "v_mfma_f32_16x16x32_bf16 v[92:95], a[172:175], a[8:11], 0\n"
    "ds_read_b64_tr_b16 a[208:209], v13 offset:36864\n"
    "v_mfma_f32_16x16x32_bf16 v[96:99], a[172:175], a[12:15], 0\n"
    "buffer_store_dwordx4 v[228:231], v123, s[52:55], s61 offen sc1\n"
    "s_add_u32 s61, s71, 3072\n"
    "v_mfma_f32_16x16x32_bf16 v[100:103], a[172:175], a[16:19], 0\n"
    "ds_read_b64_tr_b16 a[210:211], v13 offset:38912\n"
    "v_cvt_pk_bf16_f32 v232, v136, v137\n"
    "v_mfma_f32_16x16x32_bf16 v[24:27], a[176:179], a[20:23], v[24:27]\n"
    "v_cvt_pk_bf16_f32 v233, v138, v139\n"
    "v_mfma_f32_16x16x32_bf16 v[28:31], a[176:179], a[24:27], v[28:31]\n"
    "ds_read_b64_tr_b16 a[212:213], v14 offset:36864\n"
    "v_mfma_f32_16x16x32_bf16 v[32:35], a[176:179], a[28:31], v[32:35]\n"
    "v_cvt_pk_bf16_f32 v234, v156, v157\n"
    "v_mfma_f32_16x16x32_bf16 v[36:39], a[176:179], a[32:35], v[36:39]\n"
    "ds_read_b64_tr_b16 a[214:215], v14 offset:38912\n"
    "v_cvt_pk_bf16_f32 v235, v158, v159\n"
    "v_mfma_f32_16x16x32_bf16 v[40:43], a[176:179], a[36:39], v[40:43]\n"
    "v_mfma_f32_16x16x32_bf16 v[44:47], a[180:183], a[20:23], v[44:47]\n"
    "buffer_store_dwordx4 v[232:235], v123, s[52:55], s60 offen sc1\n"
    "s_add_u32 s60, s71, 4096\n"
    "v_mfma_f32_16x16x32_bf16 v[48:51], a[180:183], a[24:27], v[48:51]\n"
    "ds_read_b64_tr_b16 a[216:217], v15 offset:36864\n"
    "v_cvt_pk_bf16_f32 v236, v140, v141\n"
    "v_mfma_f32_16x16x32_bf16 v[52:55], a[180:183], a[28:31], v[52:55]\n"
    "ds_read_b64_tr_b16 a[218:219], v15 offset:38912\n"
    "v_cvt_pk_bf16_f32 v237, v142, v143\n"
    "v_mfma_f32_16x16x32_bf16 v[56:59], a[180:183], a[32:35], v[56:59]\n"
    "v_cvt_pk_bf16_f32 v238, v160, v161\n"
    "v_mfma_f32_16x16x32_bf16 v[60:63], a[180:183], a[36:39], v[60:63]\n"
    "ds_read_b64_tr_b16 a[220:221], v16 offset:36864\n"
    "v_cvt_pk_bf16_f32 v239, v162, v163\n"
    "v_mfma_f32_16x16x32_bf16 v[64:67], a[184:187], a[20:23], v[64:67]\n"
    "v_mfma_f32_16x16x32_bf16 v[68:71], a[184:187], a[24:27], v[68:71]\n"
    "ds_read_b64_tr_b16 a[222:223], v16 offset:38912\n"
    "v_mfma_f32_16x16x32_bf16 v[72:75], a[184:187], a[28:31], v[72:75]\n"
    "buffer_store_dwordx4 v[236:239], v123, s[52:55], s61 offen sc1\n"
    "v_mfma_f32_16x16x32_bf16 v[76:79], a[184:187], a[32:35], v[76:79]\n"
    "v_cvt_pk_bf16_f32 v240, v144, v145\n"
    "v_mfma_f32_16x16x32_bf16 v[80:83], a[184:187], a[36:39], v[80:83]\n"
    "v_cvt_pk_bf16_f32 v241, v146, v147\n"
    "v_mfma_f32_16x16x32_bf16 v[84:87], a[188:191], a[20:23], v[84:87]\n"
    "v_cvt_pk_bf16_f32 v242, v164, v165\n"
    "v_mfma_f32_16x16x32_bf16 v[88:91], a[188:191], a[24:27], v[88:91]\n"
    "v_cvt_pk_bf16_f32 v243, v166, v167\n"
    "v_mfma_f32_16x16x32_bf16 v[92:95], a[188:191], a[28:31], v[92:95]\n"
    "v_mfma_f32_16x16x32_bf16 v[96:99], a[188:191], a[32:35], v[96:99]\n"
    "buffer_store_dwordx4 v[240:243], v123, s[52:55], s60 offen sc1\n"
    "v_mfma_f32_16x16x32_bf16 v[100:103], a[188:191], a[36:39], v[100:103]\n"
    "s_waitcnt vmcnt(13) lgkmcnt(0)\n"
    "v_mfma_f32_16x16x32_bf16 v[24:27], a[192:195], a[40:43], v[24:27]\n"
    "ds_read_b64_tr_b16 a[160:161], v13 offset:0\n"
    "v_cvt_pk_bf16_f32 v244, v168, v169\n"
    "v_mfma_f32_16x16x32_bf16 v[28:31], a[192:195], a[44:47], v[28:31]\n"
    "s_add_u32 s61, s71, 5120\n"
    "s_add_u32 s60, s71, 6144\n"
    "v_cvt_pk_bf16_f32 v245, v170, v171\n"
    "v_mfma_f32_16x16x32_bf16 v[32:35], a[192:195], a[48:51], v[32:35]\n"
    "ds_read_b64_tr_b16 a[162:163], v13 offset:2048\n"
    "v_cvt_pk_bf16_f32 v246, v188, v189\n"
    "v_mfma_f32_16x16x32_bf16 v[36:39], a[192:195], a[52:55], v[36:39]\n"
    "v_mfma_f32_16x16x32_bf16 v[40:43], a[192:195], a[56:59], v[40:43]\n"
    "ds_read_b64_tr_b16 a[164:165], v14 offset:0\n"
    "v_cvt_pk_bf16_f32 v247, v190, v191\n"
    "v_mfma_f32_16x16x32_bf16 v[44:47], a[196:199], a[40:43], v[44:47]\n"
    "v_mfma_f32_16x16x32_bf16 v[48:51], a[196:199], a[44:47], v[48:51]\n"
    "buffer_store_dwordx4 v[244:247], v123, s[52:55], s61 offen sc1\n"
    "s_add_u32 s61, s71, 7168\n"
    "v_mfma_f32_16x16x32_bf16 v[52:55], a[196:199], a[48:51], v[52:55]\n"
    "ds_read_b64_tr_b16 a[166:167], v14 offset:2048\n"
    "v_mfma_f32_16x16x32_bf16 v[56:59], a[196:199], a[52:55], v[56:59]\n"
    "ds_read_b64_tr_b16 a[168:169], v15 offset:0\n"
    "v_cvt_pk_bf16_f32 v248, v172, v173\n"
    "v_mfma_f32_16x16x32_bf16 v[60:63], a[196:199], a[56:59], v[60:63]\n"
    "v_cvt_pk_bf16_f32 v249, v174, v175\n"
    "v_mfma_f32_16x16x32_bf16 v[64:67], a[200:203], a[40:43], v[64:67]\n"
    "ds_read_b64_tr_b16 a[170:171], v15 offset:2048\n"
    "v_cvt_pk_bf16_f32 v250, v192, v193\n"
    "v_mfma_f32_16x16x32_bf16 v[68:71], a[200:203], a[44:47], v[68:71]\n"
    "v_mfma_f32_16x16x32_bf16 v[72:75], a[200:203], a[48:51], v[72:75]\n"
    "ds_read_b64_tr_b16 a[172:173], v16 offset:0\n"
    "v_cvt_pk_bf16_f32 v251, v194, v195\n"
    "v_mfma_f32_16x16x32_bf16 v[76:79], a[200:203], a[52:55], v[76:79]\n"
    "v_mfma_f32_16x16x32_bf16 v[80:83], a[200:203], a[56:59], v[80:83]\n"
    "buffer_store_dwordx4 v[248:251], v123, s[52:55], s60 offen sc1\n"
    "s_add_u32 s60, s71, 8192\n"
    "v_mfma_f32_16x16x32_bf16 v[84:87], a[204:207], a[40:43], v[84:87]\n"
    "ds_read_b64_tr_b16 a[174:175], v16 offset:2048\n"
    "v_mfma_f32_16x16x32_bf16 v[88:91], a[204:207], a[44:47], v[88:91]\n"
    "v_cvt_pk_bf16_f32 v252, v176, v177\n"
    "v_mfma_f32_16x16x32_bf16 v[92:95], a[204:207], a[48:51], v[92:95]\n"
    "ds_read_b64_tr_b16 a[176:177], v13 offset:4096\n"
    "v_cvt_pk_bf16_f32 v253, v178, v179\n"
    "v_mfma_f32_16x16x32_bf16 v[96:99], a[204:207], a[52:55], v[96:99]\n"
    "v_cvt_pk_bf16_f32 v254, v196, v197\n"
    "v_mfma_f32_16x16x32_bf16 v[100:103], a[204:207], a[56:59], v[100:103]\n"
    "ds_read_b64_tr_b16 a[178:179], v13 offset:6144\n"
    "v_mfma_f32_16x16x32_bf16 v[24:27], a[208:211], a[60:63], v[24:27]\n"
    "v_cvt_pk_bf16_f32 v255, v198, v199\n"
    "v_mfma_f32_16x16x32_bf16 v[28:31], a[208:211], a[64:67], v[28:31]\n"
    "ds_read_b64_tr_b16 a[180:181], v14 offset:4096\n"
    "v_mfma_f32_16x16x32_bf16 v[32:35], a[208:211], a[68:71], v[32:35]\n"
    "buffer_store_dwordx4 v[252:255], v123, s[52:55], s61 offen sc1\n"
    "s_add_u32 s61, s71, 9216\n"
    "v_mfma_f32_16x16x32_bf16 v[36:39], a[208:211], a[72:75], v[36:39]\n"
    "ds_read_b64_tr_b16 a[182:183], v14 offset:6144\n"
    "v_mfma_f32_16x16x32_bf16 v[40:43], a[208:211], a[76:79], v[40:43]\n"
    "v_cvt_pk_bf16_f32 v224, v180, v181\n"
    "v_mfma_f32_16x16x32_bf16 v[44:47], a[212:215], a[60:63], v[44:47]\n"
    "ds_read_b64_tr_b16 a[184:185], v15 offset:4096\n"
    "v_cvt_pk_bf16_f32 v225, v182, v183\n"
    "v_mfma_f32_16x16x32_bf16 v[48:51], a[212:215], a[64:67], v[48:51]\n"
    "v_cvt_pk_bf16_f32 v226, v200, v201\n"
    "v_mfma_f32_16x16x32_bf16 v[52:55], a[212:215], a[68:71], v[52:55]\n"
    "ds_read_b64_tr_b16 a[186:187], v15 offset:6144\n"
    "v_mfma_f32_16x16x32_bf16 v[56:59], a[212:215], a[72:75], v[56:59]\n"
    "v_cvt_pk_bf16_f32 v227, v202, v203\n"
    "v_mfma_f32_16x16x32_bf16 v[60:63], a[212:215], a[76:79], v[60:63]\n"
    "ds_read_b64_tr_b16 a[188:189], v16 offset:4096\n"
    "v_mfma_f32_16x16x32_bf16 v[64:67], a[216:219], a[60:63], v[64:67]\n"
    "buffer_store_dwordx4 v[224:227], v123, s[52:55], s60 offen sc1\n"
    "v_mfma_f32_16x16x32_bf16 v[68:71], a[216:219], a[64:67], v[68:71]\n"
    "ds_read_b64_tr_b16 a[190:191], v16 offset:6144\n"
    "v_mfma_f32_16x16x32_bf16 v[72:75], a[216:219], a[68:71], v[72:75]\n"
    "v_cvt_pk_bf16_f32 v228, v184, v185\n"
    "v_mfma_f32_16x16x32_bf16 v[76:79], a[216:219], a[72:75], v[76:79]\n"
    "v_cvt_pk_bf16_f32 v229, v186, v187\n"
    "v_mfma_f32_16x16x32_bf16 v[80:83], a[216:219], a[76:79], v[80:83]\n"
    "v_cvt_pk_bf16_f32 v230, v204, v205\n"
    "v_mfma_f32_16x16x32_bf16 v[84:87], a[220:223], a[60:63], v[84:87]\n"
    "v_mfma_f32_16x16x32_bf16 v[88:91], a[220:223], a[64:67], v[88:91]\n"
    "v_cvt_pk_bf16_f32 v231, v206, v207\n"
    "v_mfma_f32_16x16x32_bf16 v[92:95], a[220:223], a[68:71], v[92:95]\n"
    "v_mfma_f32_16x16x32_bf16 v[96:99], a[220:223], a[72:75], v[96:99]\n"
    "buffer_store_dwordx4 v[228:231], v123, s[52:55], s61 offen sc1\n"
    "v_mfma_f32_16x16x32_bf16 v[100:103], a[220:223], a[76:79], v[100:103]\n"
    "s_waitcnt vmcnt(10) lgkmcnt(0)\n"
    "v_mfma_f32_16x16x32_bf16 v[24:27], a[160:163], a[80:83], v[24:27]\n"
    "ds_read_b64_tr_b16 a[192:193], v13 offset:8192\n"
    "v_mfma_f32_16x16x32_bf16 v[28:31], a[160:163], a[84:87], v[28:31]\n"
    "v_mfma_f32_16x16x32_bf16 v[32:35], a[160:163], a[88:91], v[32:35]\n"
    "ds_read_b64_tr_b16 a[194:195], v13 offset:10240\n"
    "v_mfma_f32_16x16x32_bf16 v[36:39], a[160:163], a[92:95], v[36:39]\n"
    "v_mfma_f32_16x16x32_bf16 v[40:43], a[160:163], a[96:99], v[40:43]\n"
    "ds_read_b64_tr_b16 a[196:197], v14 offset:8192\n"
    "v_mfma_f32_16x16x32_bf16 v[44:47], a[164:167], a[80:83], v[44:47]\n"
    "v_mfma_f32_16x16x32_bf16 v[48:51], a[164:167], a[84:87], v[48:51]\n"
    "ds_read_b64_tr_b16 a[198:199], v14 offset:10240\n"
    "v_mfma_f32_16x16x32_bf16 v[52:55], a[164:167], a[88:91], v[52:55]\n"
    "v_mfma_f32_16x16x32_bf16 v[56:59], a[164:167], a[92:95], v[56:59]\n"
    "ds_read_b64_tr_b16 a[200:201], v15 offset:8192\n"
    "v_mfma_f32_16x16x32_bf16 v[60:63], a[164:167], a[96:99], v[60:63]\n"
    "v_mfma_f32_16x16x32_bf16 v[64:67], a[168:171], a[80:83], v[64:67]\n"
    "ds_read_b64_tr_b16 a[202:203], v15 offset:10240\n"
    "v_mfma_f32_16x16x32_bf16 v[68:71], a[168:171], a[84:87], v[68:71]\n"
    "v_mfma_f32_16x16x32_bf16 v[72:75], a[168:171], a[88:91], v[72:75]\n"
    "ds_read_b64_tr_b16 a[204:205], v16 offset:8192\n"
    "v_mfma_f32_16x16x32_bf16 v[76:79], a[168:171], a[92:95], v[76:79]\n"
    "v_mfma_f32_16x16x32_bf16 v[80:83], a[168:171], a[96:99], v[80:83]\n"
    "ds_read_b64_tr_b16 a[206:207], v16 offset:10240\n"
    "v_mfma_f32_16x16x32_bf16 v[84:87], a[172:175], a[80:83], v[84:87]\n"
    "v_mfma_f32_16x16x32_bf16 v[88:91], a[172:175], a[84:87], v[88:91]\n"
    "v_mfma_f32_16x16x32_bf16 v[92:95], a[172:175], a[88:91], v[92:95]\n"
    "ds_read_b64_tr_b16 a[208:209], v13 offset:12288\n"
    "v_mfma_f32_16x16x32_bf16 v[96:99], a[172:175], a[92:95], v[96:99]\n"
    "v_mfma_f32_16x16x32_bf16 v[100:103], a[172:175], a[96:99], v[100:103]\n"
    "ds_read_b64_tr_b16 a[210:211], v13 offset:14336\n"
    "v_mfma_f32_16x16x32_bf16 v[24:27], a[176:179], a[100:103], v[24:27]\n"
    "v_mfma_f32_16x16x32_bf16 v[28:31], a[176:179], a[104:107], v[28:31]\n"
    "ds_read_b64_tr_b16 a[212:213], v14 offset:12288\n"
    "v_mfma_f32_16x16x32_bf16 v[32:35], a[176:179], a[108:111], v[32:35]\n"
    "v_mfma_f32_16x16x32_bf16 v[36:39], a[176:179], a[112:115], v[36:39]\n"
    "ds_read_b64_tr_b16 a[214:215], v14 offset:14336\n"
    "v_mfma_f32_16x16x32_bf16 v[40:43], a[176:179], a[116:119], v[40:43]\n"
    "v_mfma_f32_16x16x32_bf16 v[44:47], a[180:183], a[100:103], v[44:47]\n"
    "ds_read_b64_tr_b16 a[216:217], v15 offset:12288\n"
    "v_mfma_f32_16x16x32_bf16 v[48:51], a[180:183], a[104:107], v[48:51]\n"
    "v_mfma_f32_16x16x32_bf16 v[52:55], a[180:183], a[108:111], v[52:55]\n"
    "ds_read_b64_tr_b16 a[218:219], v15 offset:14336\n"
    "v_mfma_f32_16x16x32_bf16 v[56:59], a[180:183], a[112:115], v[56:59]\n"
    "v_mfma_f32_16x16x32_bf16 v[60:63], a[180:183], a[116:119], v[60:63]\n"
    "ds_read_b64_tr_b16 a[220:221], v16 offset:12288\n"
    "v_mfma_f32_16x16x32_bf16 v[64:67], a[184:187], a[100:103], v[64:67]\n"
    "v_mfma_f32_16x16x32_bf16 v[68:71], a[184:187], a[104:107], v[68:71]\n"
    "ds_read_b64_tr_b16 a[222:223], v16 offset:14336\n"
    "v_mfma_f32_16x16x32_bf16 v[72:75], a[184:187], a[108:111], v[72:75]\n"
    "v_mfma_f32_16x16x32_bf16 v[76:79], a[184:187], a[112:115], v[76:79]\n"
    "v_mfma_f32_16x16x32_bf16 v[80:83], a[184:187], a[116:119], v[80:83]\n"
    "v_mfma_f32_16x16x32_bf16 v[84:87], a[188:191], a[100:103], v[84:87]\n"
    "v_mfma_f32_16x16x32_bf16 v[88:91], a[188:191], a[104:107], v[88:91]\n"
    "v_mfma_f32_16x16x32_bf16 v[92:95], a[188:191], a[108:111], v[92:95]\n"
    "v_mfma_f32_16x16x32_bf16 v[96:99], a[188:191], a[112:115], v[96:99]\n"
    "v_mfma_f32_16x16x32_bf16 v[100:103], a[188:191], a[116:119], v[100:103]\n"
    "s_waitcnt lgkmcnt(0)\n"
    "s_waitcnt vmcnt(0)\n"
    "buffer_store_dwordx2 v[10:11], v9, s[36:39], s58 offen sc1\n"
    "s_lshr_b32 s71, s20, 2\n"
    "s_xor_b32 s71, s71, 1\n"
    "s_mul_i32 s71, s71, 10240\n"
    "v_mfma_f32_16x16x32_bf16 v[24:27], a[192:195], a[120:123], v[24:27]\n"
    "s_add_u32 s60, s71, 0\n"
    "s_add_u32 s61, s71, 1024\n"
    "v_mfma_f32_16x16x32_bf16 v[44:47], a[196:199], a[120:123], v[44:47]\n"
    "v_mfma_f32_16x16x32_bf16 v[24:27], a[208:211], a[140:143], v[24:27]\n"
    "v_mfma_f32_16x16x32_bf16 v[44:47], a[212:215], a[140:143], v[44:47]\n"
    "v_mfma_f32_16x16x32_bf16 v[28:31], a[192:195], a[124:127], v[28:31]\n"
    "v_mfma_f32_16x16x32_bf16 v[48:51], a[196:199], a[124:127], v[48:51]\n"
    "v_mfma_f32_16x16x32_bf16 v[28:31], a[208:211], a[144:147], v[28:31]\n"
    "v_mfma_f32_16x16x32_bf16 v[48:51], a[212:215], a[144:147], v[48:51]\n"
    "v_mfma_f32_16x16x32_bf16 v[32:35], a[192:195], a[128:131], v[32:35]\n"
    "v_mfma_f32_16x16x32_bf16 v[52:55], a[196:199], a[128:131], v[52:55]\n"
    "v_mfma_f32_16x16x32_bf16 v[32:35], a[208:211], a[148:151], v[32:35]\n"
    "v_cvt_pk_bf16_f32 v224, v24, v25\n"
    "v_cvt_pk_bf16_f32 v225, v26, v27\n"
    "v_mfma_f32_16x16x32_bf16 v[52:55], a[212:215], a[148:151], v[52:55]\n"
    "v_cvt_pk_bf16_f32 v226, v44, v45\n"
    "v_cvt_pk_bf16_f32 v227, v46, v47\n"
    "v_mfma_f32_16x16x32_bf16 v[36:39], a[192:195], a[132:135], v[36:39]\n"
    "buffer_store_dwordx4 v[224:227], v123, s[52:55], s60 offen sc1\n"
    "v_cvt_pk_bf16_f32 v228, v28, v29\n"
    "v_mfma_f32_16x16x32_bf16 v[56:59], a[196:199], a[132:135], v[56:59]\n"
    "s_add_u32 s60, s71, 2048\n"
    "v_cvt_pk_bf16_f32 v229, v30, v31\n"
    "v_cvt_pk_bf16_f32 v230, v48, v49\n"
    "v_mfma_f32_16x16x32_bf16 v[36:39], a[208:211], a[152:155], v[36:39]\n"
    "v_cvt_pk_bf16_f32 v231, v50, v51\n"
    "v_mfma_f32_16x16x32_bf16 v[56:59], a[212:215], a[152:155], v[56:59]\n"
    "buffer_store_dwordx4 v[228:231], v123, s[52:55], s61 offen sc1\n"
    "v_cvt_pk_bf16_f32 v232, v32, v33\n"
    "v_mfma_f32_16x16x32_bf16 v[40:43], a[192:195], a[136:139], v[40:43]\n"
    "s_add_u32 s61, s71, 3072\n"
    "v_cvt_pk_bf16_f32 v233, v34, v35\n"
    "v_cvt_pk_bf16_f32 v234, v52, v53\n"
    "v_mfma_f32_16x16x32_bf16 v[60:63], a[196:199], a[136:139], v[60:63]\n"
    "v_cvt_pk_bf16_f32 v235, v54, v55\n"
    "v_mfma_f32_16x16x32_bf16 v[40:43], a[208:211], a[156:159], v[40:43]\n"
    "buffer_store_dwordx4 v[232:235], v123, s[52:55], s60 offen sc1\n"
    "s_add_u32 s60, s71, 4096\n"
    "v_mfma_f32_16x16x32_bf16 v[60:63], a[212:215], a[156:159], v[60:63]\n"
    "v_cvt_pk_bf16_f32 v236, v36, v37\n"
    "v_cvt_pk_bf16_f32 v237, v38, v39\n"
    "v_mfma_f32_16x16x32_bf16 v[64:67], a[200:203], a[120:123], v[64:67]\n"
    "v_cvt_pk_bf16_f32 v238, v56, v57\n"
    "v_mfma_f32_16x16x32_bf16 v[84:87], a[204:207], a[120:123], v[84:87]\n"
    "v_cvt_pk_bf16_f32 v239, v58, v59\n"
    "v_mfma_f32_16x16x32_bf16 v[64:67], a[216:219], a[140:143], v[64:67]\n"
    "buffer_store_dwordx4 v[236:239], v123, s[52:55], s61 offen sc1\n"
    "s_add_u32 s61, s71, 5120\n"
    "v_mfma_f32_16x16x32_bf16 v[84:87], a[220:223], a[140:143], v[84:87]\n"
    "v_cvt_pk_bf16_f32 v240, v40, v41\n"
    "v_cvt_pk_bf16_f32 v241, v42, v43\n"
    "v_mfma_f32_16x16x32_bf16 v[68:71], a[200:203], a[124:127], v[68:71]\n"
    "v_cvt_pk_bf16_f32 v242, v60, v61\n"
    "v_mfma_f32_16x16x32_bf16 v[88:91], a[204:207], a[124:127], v[88:91]\n"
    "v_cvt_pk_bf16_f32 v243, v62, v63\n"
    "v_mfma_f32_16x16x32_bf16 v[68:71], a[216:219], a[144:147], v[68:71]\n"
    "buffer_store_dwordx4 v[240:243], v123, s[52:55], s60 offen sc1\n"
    "s_add_u32 s60, s71, 6144\n"
    "v_mfma_f32_16x16x32_bf16 v[88:91], a[220:223], a[144:147], v[88:91]\n"
    "v_cvt_pk_bf16_f32 v244, v64, v65\n"
    "v_cvt_pk_bf16_f32 v245, v66, v67\n"
    "v_mfma_f32_16x16x32_bf16 v[72:75], a[200:203], a[128:131], v[72:75]\n"
    "v_cvt_pk_bf16_f32 v246, v84, v85\n"
    "v_mfma_f32_16x16x32_bf16 v[92:95], a[204:207], a[128:131], v[92:95]\n"
    "v_cvt_pk_bf16_f32 v247, v86, v87\n"
    "v_mfma_f32_16x16x32_bf16 v[72:75], a[216:219], a[148:151], v[72:75]\n"
    "buffer_store_dwordx4 v[244:247], v123, s[52:55], s61 offen sc1\n"
    "s_add_u32 s61, s71, 7168\n"
    "v_mfma_f32_16x16x32_bf16 v[92:95], a[220:223], a[148:151], v[92:95]\n"
    "v_cvt_pk_bf16_f32 v248, v68, v69\n"
    "v_cvt_pk_bf16_f32 v249, v70, v71\n"
    "v_mfma_f32_16x16x32_bf16 v[76:79], a[200:203], a[132:135], v[76:79]\n"
    "v_cvt_pk_bf16_f32 v250, v88, v89\n"
    "v_mfma_f32_16x16x32_bf16 v[96:99], a[204:207], a[132:135], v[96:99]\n"
    "v_cvt_pk_bf16_f32 v251, v90, v91\n"
    "v_mfma_f32_16x16x32_bf16 v[76:79], a[216:219], a[152:155], v[76:79]\n"
    "buffer_store_dwordx4 v[248:251], v123, s[52:55], s60 offen sc1\n"
    "v_mfma_f32_16x16x32_bf16 v[96:99], a[220:223], a[152:155], v[96:99]\n"
    "v_cvt_pk_bf16_f32 v252, v72, v73\n"
    "v_cvt_pk_bf16_f32 v253, v74, v75\n"
    "v_mfma_f32_16x16x32_bf16 v[80:83], a[200:203], a[136:139], v[80:83]\n"
    "v_cvt_pk_bf16_f32 v254, v92, v93\n"
    "v_mfma_f32_16x16x32_bf16 v[100:103], a[204:207], a[136:139], v[100:103]\n"
    "v_cvt_pk_bf16_f32 v255, v94, v95\n"
    "v_mfma_f32_16x16x32_bf16 v[80:83], a[216:219], a[156:159], v[80:83]\n"
    "buffer_store_dwordx4 v[252:255], v123, s[52:55], s61 offen sc1\n"
    "v_mfma_f32_16x16x32_bf16 v[100:103], a[220:223], a[156:159], v[100:103]\n"
    "v_cvt_pk_bf16_f32 v224, v76, v77\n"
    "v_cvt_pk_bf16_f32 v225, v78, v79\n"
    "v_cvt_pk_bf16_f32 v226, v96, v97\n"
    "v_cvt_pk_bf16_f32 v227, v98, v99\n"
    "s_add_u32 s60, s71, 8192\n"
    "buffer_store_dwordx4 v[224:227], v123, s[52:55], s60 offen sc1\n"
    "v_cvt_pk_bf16_f32 v228, v80, v81\n"
    "v_cvt_pk_bf16_f32 v229, v82, v83\n"
    "v_cvt_pk_bf16_f32 v230, v100, v101\n"
    "v_cvt_pk_bf16_f32 v231, v102, v103\n"
    "s_add_u32 s61, s71, 9216\n"
    "buffer_store_dwordx4 v[228:231], v123, s[52:55], s61 offen sc1\n"
    "s_waitcnt vmcnt(0)\n"
    "buffer_store_dwordx2 v[10:11], v9, s[36:39], s59 offen sc1\n"
    "s_cmp_eq_u32 s34, 0\n"
    "s_cbranch_scc0 LEND%=\n"
    "s_lshl_b32 s66, s20, 15\n"
    "s_lshl_b32 s67, s21, 18\n"
    "s_add_u32 s66, s66, s67\n"
    "s_lshl_b32 s67, %2, 13\n"
    "s_add_u32 s66, s66, s67\n"
    "s_add_u32 s48, s18, s66\n"
    "s_addc_u32 s49, s19, 0\n"
    "s_and_b32 s49, s49, 0xffff\n"
    "s_mov_b32 s50, 8192\n"
    "s_mov_b32 s51, 0x20000\n"
    "v_lshlrev_b32 v122, 4, v0\n"
    "s_movk_i32 s67, 0x1000\n"
    "buffer_load_dwordx4 a[252:255], v122, s[48:51], 0 offen offset:0\n"
    "buffer_load_dwordx4 a[252:255], v122, s[48:51], 0 offen offset:1024\n"
    "buffer_load_dwordx4 a[252:255], v122, s[48:51], 0 offen offset:2048\n"
    "buffer_load_dwordx4 a[252:255], v122, s[48:51], 0 offen offset:3072\n"
    "buffer_load_dwordx4 a[252:255], v122, s[48:51], s67 offen offset:0\n"
    "buffer_load_dwordx4 a[252:255], v122, s[48:51], s67 offen offset:1024\n"
    "buffer_load_dwordx4 a[252:255], v122, s[48:51], s67 offen offset:2048\n"
    "buffer_load_dwordx4 a[252:255], v122, s[48:51], s67 offen offset:3072\n"
    "s_cmp_eq_u32 %1, 0\n"
    "s_cbranch_scc0 LNOBUMP%=\n"
    "s_cmp_eq_u32 %2, 3\n"
    "s_cbranch_scc0 LNOBUMP%=\n"
    "s_add_u32 s48, s14, 67112960\n"
    "s_addc_u32 s49, s15, 0\n"
    "s_and_b32 s49, s49, 0xffff\n"
    "s_mov_b32 s50, 2048\n"
    "s_mov_b32 s51, 0x20000\n"
    "s_mov_b32 s62, 0\n"
    "LBPOLL%=:\n"
    "buffer_load_dwordx4 v[24:27], v123, s[48:51], 0 offen sc1\n"
    "buffer_load_dwordx4 v[28:31], v123, s[48:51], 0 offen offset:1024 sc1\n"
    "s_waitcnt vmcnt(0)\n"
    "v_cmp_eq_u64 vcc, s[28:29], v[24:25]\n"
    "s_nop 0\n"
    "s_mov_b64 s[72:73], vcc\n"
    "v_cmp_eq_u64 vcc, s[28:29], v[26:27]\n"
    "s_nop 0\n"
    "s_and_b64 s[72:73], s[72:73], vcc\n"
    "v_cmp_eq_u64 vcc, s[28:29], v[28:29]\n"
    "s_nop 0\n"
    "s_and_b64 s[72:73], s[72:73], vcc\n"
    "v_cmp_eq_u64 vcc, s[28:29], v[30:31]\n"
    "s_nop 0\n"
    "s_and_b64 s[72:73], s[72:73], vcc\n"
    "s_cmp_eq_u64 s[72:73], -1\n"
    "s_cbranch_scc1 LBGO%=\n"
    "s_add_u32 s62, s62, 1\n"
    "s_cmp_lt_u32 s62, 262144\n"
    "s_cbranch_scc0 LBGO%=\n"
    "s_sleep 8\n"
    "s_branch LBPOLL%=\n"
    "LBGO%=:\n"
    "s_add_u32 s48, s14, 67108864\n"
    "s_addc_u32 s49, s15, 0\n"
    "s_and_b32 s49, s49, 0xffff\n"
    "s_mov_b32 s50, 8\n"
    "s_mov_b32 s51, 0x20000\n"
    "s_add_u32 s64, s26, 1\n"
    "s_addc_u32 s65, s27, 0\n"
    "v_mov_b32 v24, s64\n"
    "v_mov_b32 v25, s65\n"
    "buffer_store_dwordx2 v[24:25], v9, s[48:51], 0 offen sc1\n"
    "LNOBUMP%=:\n"
    "v_lshrrev_b32 v2, 4, v0\n"
    "v_and_b32 v3, 15, v0\n"
    "v_lshrrev_b32 v4, 2, v3\n"
    "v_lshlrev_b32 v4, 3, v4\n"
    "v_and_b32 v5, 3, v3\n"
    "v_add3_u32 v4, v4, v5, 0\n"
    "v_lshrrev_b32 v6, 4, v4\n"
    "v_lshl_add_u32 v5, v6, 2, v5\n"
    "v_bfe_u32 v6, v4, 2, 2\n"
    "v_and_b32 v7, 1, v5\n"
    "v_lshlrev_b32 v7, 4, v7\n"
    "v_mov_b32 v8, 0x3f80\n"
    "v_lshlrev_b32 v7, v7, v8\n"
    "v_cmp_eq_u32 vcc, v6, v2\n"
    "s_nop 1\n"
    "v_cndmask_b32 v7, 0, v7, vcc\n"
    "v_lshrrev_b32 v6, 1, v5\n"
    "v_cmp_eq_u32 vcc, 0, v6\n"
    "s_nop 1\n"
    "v_cndmask_b32 v108, 0, v7, vcc\n"
    "v_cmp_eq_u32 vcc, 1, v6\n"
    "s_nop 1\n"
    "v_cndmask_b32 v109, 0, v7, vcc\n"
    "v_cmp_eq_u32 vcc, 2, v6\n"
    "s_nop 1\n"
    "v_cndmask_b32 v110, 0, v7, vcc\n"
    "v_cmp_eq_u32 vcc, 3, v6\n"
    "s_nop 1\n"
    "v_cndmask_b32 v111, 0, v7, vcc\n"
    "v_lshrrev_b32 v4, 2, v3\n"
    "v_lshlrev_b32 v4, 3, v4\n"
    "v_and_b32 v5, 3, v3\n"
    "v_add3_u32 v4, v4, v5, 4\n"
    "v_lshrrev_b32 v6, 4, v4\n"
    "v_lshl_add_u32 v5, v6, 2, v5\n"
    "v_bfe_u32 v6, v4, 2, 2\n"
    "v_and_b32 v7, 1, v5\n"
    "v_lshlrev_b32 v7, 4, v7\n"
    "v_mov_b32 v8, 0x3f80\n"
    "v_lshlrev_b32 v7, v7, v8\n"
    "v_cmp_eq_u32 vcc, v6, v2\n"
    "s_nop 1\n"
    "v_cndmask_b32 v7, 0, v7, vcc\n"
    "v_lshrrev_b32 v6, 1, v5\n"
    "v_cmp_eq_u32 vcc, 0, v6\n"
    "s_nop 1\n"
    "v_cndmask_b32 v112, 0, v7, vcc\n"
    "v_cmp_eq_u32 vcc, 1, v6\n"
    "s_nop 1\n"
    "v_cndmask_b32 v113, 0, v7, vcc\n"
    "v_cmp_eq_u32 vcc, 2, v6\n"
    "s_nop 1\n"
    "v_cndmask_b32 v114, 0, v7, vcc\n"
    "v_cmp_eq_u32 vcc, 3, v6\n"
    "s_nop 1\n"
    "v_cndmask_b32 v115, 0, v7, vcc\n"
    "s_mul_i32 s64, s21, 5120\n"
    "s_lshl_b32 s65, %2, 10\n"
    "s_add_u32 s65, s65, s64\n"
    "v_add_u32 v116, s65, v123\n"
    "s_add_u32 s64, s64, 4096\n"
    "s_cmp_eq_u32 %2, 0\n"
    "s_cselect_b32 s64, s64, 0x3ff80000\n"
    "v_add_u32 v117, s64, v123\n"
    "v_and_b32 v118, 31, v0\n"
    "v_lshlrev_b32 v118, 3, v118\n"
    "v_lshlrev_b32 v119, 11, v3\n"
    "v_lshl_add_u32 v119, v2, 4, v119\n"
    "s_lshl_b32 s64, s21, 6\n"
    "s_add_u32 s66, s64, 131072\n"
    "s_cmp_eq_u32 %2, 0\n"
    "s_cselect_b32 s66, s66, 0x3ff80000\n"
    "v_add_u32 v120, s66, v119\n"
    "s_lshl_b32 s65, %2, 15\n"
    "s_add_u32 s64, s64, s65\n"
    "v_add_u32 v119, s64, v119\n"
    "s_mul_i32 s64, s20, 163840\n"
    "s_add_u32 s52, s16, s64\n"
    "s_addc_u32 s53, s17, 0\n"
    "s_and_b32 s53, s53, 0xffff\n"
    "s_mov_b32 s54, 163840\n"
    "s_mov_b32 s55, 0x20000\n"
    "s_lshl_b32 s65, s20, 3\n"
    "s_lshr_b32 s64, s21, 1\n"
    "s_add_u32 s65, s65, s64\n"
    "s_lshl_b32 s65, s65, 8\n"
    "s_add_u32 s65, s65, 67174400\n"
    "s_add_u32 s48, s14, s65\n"
    "s_addc_u32 s49, s15, 0\n"
    "s_and_b32 s49, s49, 0xffff\n"
    "s_mov_b32 s50, 256\n"
    "s_mov_b32 s51, 0x20000\n"
    "s_mul_i32 s64, s20, 2621440\n"
    "s_add_u32 s44, s14, s64\n"
    "s_addc_u32 s45, s15, 0\n"
    "s_and_b32 s45, s45, 0xffff\n"
    "s_mov_b32 s46, 2621440\n"
    "s_mov_b32 s47, 0x20000\n"
    "s_getpc_b64 s[36:37]\n"
    "LRPC%=:\n"
    "s_add_u32 s38, s36, LRSUM%=-LRPC%=\n"
    "s_addc_u32 s39, s37, 0\n"
    "s_add_u32 s36, s36, LRTAB%=-LRPC%=\n"
    "s_addc_u32 s37, s37, 0\n"
    "s_mov_b32 s76, 0\n"
    "s_mov_b32 s77, 0\n"
    "s_mov_b32 s62, 0\n"
    "s_mov_b32 s71, 0\n"
    "LRINIT%=:\n"
    "v_mov_b32 v32, 0\n"
    "v_mov_b32 v33, 0\n"
    "v_mov_b32 v34, 0\n"
    "v_mov_b32 v35, 0\n"
    "v_mov_b32 v36, 0\n"
    "v_mov_b32 v37, 0\n"
    "v_mov_b32 v38, 0\n"
    "v_mov_b32 v39, 0\n"
    "v_mov_b32 v40, 0\n"
    "v_mov_b32 v41, 0\n"
    "v_mov_b32 v42, 0\n"
    "v_mov_b32 v43, 0\n"
    "v_mov_b32 v44, 0\n"
    "v_mov_b32 v45, 0\n"
    "v_mov_b32 v46, 0\n"
    "v_mov_b32 v47, 0\n"
    "s_mov_b32 s58, 0xffff\n"
    "s_mov_b32 s70, 0\n"
    "LRPOLL%=:\n"
    "buffer_load_dwordx2 v[24:25], v118, s[48:51], 0 offen sc1\n"
    "LRNP%=:\n"
    "s_add_u32 s62, s62, 1\n"
    "s_ff0_i32_b32 s66, s70\n"
    "s_mul_i32 s66, s66, 40\n"
    "s_add_u32 s40, s38, s66\n"
    "s_addc_u32 s41, s39, 0\n"
    "s_xor_b32 s70, s58, 0xffff\n"
    "s_waitcnt vmcnt(0)\n"
    "v_cmp_eq_u64 vcc, s[28:29], v[24:25]\n"
    "s_cmp_lt_u32 s62, 262144\n"
    "s_cselect_b32 s75, vcc_lo, -1\n"
    "s_lshr_b32 s75, s75, s76\n"
    "s_and_b32 s75, s75, s58\n"
    "s_cbranch_scc0 LRIX%=\n"
    "s_andn2_b32 s58, s58, s75\n"
    "LRIL%=:\n"
    "s_ff1_i32_b32 s66, s75\n"
    "s_bitset0_b32 s75, s66\n"
    "s_mul_i32 s67, s66, 81920\n"
    "s_add_u32 s67, s67, s71\n"
    "s_mul_i32 s66, s66, 24\n"
    "s_add_u32 s64, s36, s66\n"
    "s_addc_u32 s65, s37, 0\n"
    "s_cmp_eq_u32 s75, 0\n"
    "s_setpc_b64 s[64:65]\n"
    "LRTAB%=:\n"
    "buffer_load_dwordx4 a[0:3], v116, s[44:47], s67 offen sc1\n"
    "buffer_load_dwordx4 a[4:7], v117, s[44:47], s67 offen sc1\n"
    "s_cbranch_scc0 LRIL%=\n"
    "s_setpc_b64 s[40:41]\n"
    "buffer_load_dwordx4 a[8:11], v116, s[44:47], s67 offen sc1\n"
    "buffer_load_dwordx4 a[12:15], v117, s[44:47], s67 offen sc1\n"
    "s_cbranch_scc0 LRIL%=\n"
    "s_setpc_b64 s[40:41]\n"
    "buffer_load_dwordx4 a[16:19], v116, s[44:47], s67 offen sc1\n"
    "buffer_load_dwordx4 a[20:23], v117, s[44:47], s67 offen sc1\n"
    "s_cbranch_scc0 LRIL%=\n"
    "s_setpc_b64 s[40:41]\n"
    "buffer_load_dwordx4 a[24:27], v116, s[44:47], s67 offen sc1\n"
    "buffer_load_dwordx4 a[28:31], v117, s[44:47], s67 offen sc1\n"
    "s_cbranch_scc0 LRIL%=\n"
    "s_setpc_b64 s[40:41]\n"
    "buffer_load_dwordx4 a[32:35], v116, s[44:47], s67 offen sc1\n"
    "buffer_load_dwordx4 a[36:39], v117, s[44:47], s67 offen sc1\n"
    "s_cbranch_scc0 LRIL%=\n"
    "s_setpc_b64 s[40:41]\n"
    "buffer_load_dwordx4 a[40:43], v116, s[44:47], s67 offen sc1\n"
    "buffer_load_dwordx4 a[44:47], v117, s[44:47], s67 offen sc1\n"
    "s_cbranch_scc0 LRIL%=\n"
    "s_setpc_b64 s[40:41]\n"
    "buffer_load_dwordx4 a[48:51], v116, s[44:47], s67 offen sc1\n"
    "buffer_load_dwordx4 a[52:55], v117, s[44:47], s67 offen sc1\n"
    "s_cbranch_scc0 LRIL%=\n"
    "s_setpc_b64 s[40:41]\n"
    "buffer_load_dwordx4 a[56:59], v116, s[44:47], s67 offen sc1\n"
    "buffer_load_dwordx4 a[60:63], v117, s[44:47], s67 offen sc1\n"
    "s_cbranch_scc0 LRIL%=\n"
    "s_setpc_b64 s[40:41]\n"
    "buffer_load_dwordx4 a[64:67], v116, s[44:47], s67 offen sc1\n"
    "buffer_load_dwordx4 a[68:71], v117, s[44:47], s67 offen sc1\n"
    "s_cbranch_scc0 LRIL%=\n"
    "s_setpc_b64 s[40:41]\n"
    "buffer_load_dwordx4 a[72:75], v116, s[44:47], s67 offen sc1\n"
    "buffer_load_dwordx4 a[76:79], v117, s[44:47], s67 offen sc1\n"
    "s_cbranch_scc0 LRIL%=\n"
    "s_setpc_b64 s[40:41]\n"
    "buffer_load_dwordx4 a[80:83], v116, s[44:47], s67 offen sc1\n"
    "buffer_load_dwordx4 a[84:87], v117, s[44:47], s67 offen sc1\n"
    "s_cbranch_scc0 LRIL%=\n"
    "s_setpc_b64 s[40:41]\n"
    "buffer_load_dwordx4 a[88:91], v116, s[44:47], s67 offen sc1\n"
    "buffer_load_dwordx4 a[92:95], v117, s[44:47], s67 offen sc1\n"
    "s_cbranch_scc0 LRIL%=\n"
    "s_setpc_b64 s[40:41]\n"
    "buffer_load_dwordx4 a[96:99], v116, s[44:47], s67 offen sc1\n"
    "buffer_load_dwordx4 a[100:103], v117, s[44:47], s67 offen sc1\n"
    "s_cbranch_scc0 LRIL%=\n"
    "s_setpc_b64 s[40:41]\n"
    "buffer_load_dwordx4 a[104:107], v116, s[44:47], s67 offen sc1\n"
    "buffer_load_dwordx4 a[108:111], v117, s[44:47], s67 offen sc1\n"
    "s_cbranch_scc0 LRIL%=\n"
    "s_setpc_b64 s[40:41]\n"
    "buffer_load_dwordx4 a[112:115], v116, s[44:47], s67 offen sc1\n"
    "buffer_load_dwordx4 a[116:119], v117, s[44:47], s67 offen sc1\n"
    "s_cbranch_scc0 LRIL%=\n"
    "s_setpc_b64 s[40:41]\n"
    "buffer_load_dwordx4 a[120:123], v116, s[44:47], s67 offen sc1\n"
    "buffer_load_dwordx4 a[124:127], v117, s[44:47], s67 offen sc1\n"
    "s_cbranch_scc0 LRIL%=\n"
    "s_setpc_b64 s[40:41]\n"
    "LRIX%=:\n"
    "s_setpc_b64 s[40:41]\n"
    "LRSUM%=:\n"
    "s_bitcmp1_b32 s70, 0\n"
    "s_cbranch_scc0 LRAX%=\n"
    "v_mfma_f32_16x16x32_bf16 v[32:35], v[108:111], a[0:3], v[32:35]\n"
    "v_mfma_f32_16x16x32_bf16 v[36:39], v[112:115], a[0:3], v[36:39]\n"
    "v_mfma_f32_16x16x32_bf16 v[40:43], v[108:111], a[4:7], v[40:43]\n"
    "v_mfma_f32_16x16x32_bf16 v[44:47], v[112:115], a[4:7], v[44:47]\n"
    "s_bitcmp1_b32 s70, 1\n"
    "s_cbranch_scc0 LRAX%=\n"
    "v_mfma_f32_16x16x32_bf16 v[32:35], v[108:111], a[8:11], v[32:35]\n"
    "v_mfma_f32_16x16x32_bf16 v[36:39], v[112:115], a[8:11], v[36:39]\n"
    "v_mfma_f32_16x16x32_bf16 v[40:43], v[108:111], a[12:15], v[40:43]\n"
    "v_mfma_f32_16x16x32_bf16 v[44:47], v[112:115], a[12:15], v[44:47]\n"
    "s_bitcmp1_b32 s70, 2\n"
    "s_cbranch_scc0 LRAX%=\n"
    "v_mfma_f32_16x16x32_bf16 v[32:35], v[108:111], a[16:19], v[32:35]\n"
    "v_mfma_f32_16x16x32_bf16 v[36:39], v[112:115], a[16:19], v[36:39]\n"
    "v_mfma_f32_16x16x32_bf16 v[40:43], v[108:111], a[20:23], v[40:43]\n"
    "v_mfma_f32_16x16x32_bf16 v[44:47], v[112:115], a[20:23], v[44:47]\n"
    "s_bitcmp1_b32 s70, 3\n"
    "s_cbranch_scc0 LRAX%=\n"
    "v_mfma_f32_16x16x32_bf16 v[32:35], v[108:111], a[24:27], v[32:35]\n"
    "v_mfma_f32_16x16x32_bf16 v[36:39], v[112:115], a[24:27], v[36:39]\n"
    "v_mfma_f32_16x16x32_bf16 v[40:43], v[108:111], a[28:31], v[40:43]\n"
    "v_mfma_f32_16x16x32_bf16 v[44:47], v[112:115], a[28:31], v[44:47]\n"
    "s_bitcmp1_b32 s70, 4\n"
    "s_cbranch_scc0 LRAX%=\n"
    "v_mfma_f32_16x16x32_bf16 v[32:35], v[108:111], a[32:35], v[32:35]\n"
    "v_mfma_f32_16x16x32_bf16 v[36:39], v[112:115], a[32:35], v[36:39]\n"
    "v_mfma_f32_16x16x32_bf16 v[40:43], v[108:111], a[36:39], v[40:43]\n"
    "v_mfma_f32_16x16x32_bf16 v[44:47], v[112:115], a[36:39], v[44:47]\n"
    "s_bitcmp1_b32 s70, 5\n"
    "s_cbranch_scc0 LRAX%=\n"
    "v_mfma_f32_16x16x32_bf16 v[32:35], v[108:111], a[40:43], v[32:35]\n"
    "v_mfma_f32_16x16x32_bf16 v[36:39], v[112:115], a[40:43], v[36:39]\n"
    "v_mfma_f32_16x16x32_bf16 v[40:43], v[108:111], a[44:47], v[40:43]\n"
    "v_mfma_f32_16x16x32_bf16 v[44:47], v[112:115], a[44:47], v[44:47]\n"
    "s_bitcmp1_b32 s70, 6\n"
    "s_cbranch_scc0 LRAX%=\n"
    "v_mfma_f32_16x16x32_bf16 v[32:35], v[108:111], a[48:51], v[32:35]\n"
    "v_mfma_f32_16x16x32_bf16 v[36:39], v[112:115], a[48:51], v[36:39]\n"
    "v_mfma_f32_16x16x32_bf16 v[40:43], v[108:111], a[52:55], v[40:43]\n"
    "v_mfma_f32_16x16x32_bf16 v[44:47], v[112:115], a[52:55], v[44:47]\n"
    "s_bitcmp1_b32 s70, 7\n"
    "s_cbranch_scc0 LRAX%=\n"
    "v_mfma_f32_16x16x32_bf16 v[32:35], v[108:111], a[56:59], v[32:35]\n"
    "v_mfma_f32_16x16x32_bf16 v[36:39], v[112:115], a[56:59], v[36:39]\n"
    "v_mfma_f32_16x16x32_bf16 v[40:43], v[108:111], a[60:63], v[40:43]\n"
    "v_mfma_f32_16x16x32_bf16 v[44:47], v[112:115], a[60:63], v[44:47]\n"
    "s_bitcmp1_b32 s70, 8\n"
    "s_cbranch_scc0 LRAX%=\n"
    "v_mfma_f32_16x16x32_bf16 v[32:35], v[108:111], a[64:67], v[32:35]\n"
    "v_mfma_f32_16x16x32_bf16 v[36:39], v[112:115], a[64:67], v[36:39]\n"
    "v_mfma_f32_16x16x32_bf16 v[40:43], v[108:111], a[68:71], v[40:43]\n"
    "v_mfma_f32_16x16x32_bf16 v[44:47], v[112:115], a[68:71], v[44:47]\n"
    "s_bitcmp1_b32 s70, 9\n"
    "s_cbranch_scc0 LRAX%=\n"
    "v_mfma_f32_16x16x32_bf16 v[32:35], v[108:111], a[72:75], v[32:35]\n"
    "v_mfma_f32_16x16x32_bf16 v[36:39], v[112:115], a[72:75], v[36:39]\n"
    "v_mfma_f32_16x16x32_bf16 v[40:43], v[108:111], a[76:79], v[40:43]\n"
    "v_mfma_f32_16x16x32_bf16 v[44:47], v[112:115], a[76:79], v[44:47]\n"
    "s_bitcmp1_b32 s70, 10\n"
    "s_cbranch_scc0 LRAX%=\n"
    "v_mfma_f32_16x16x32_bf16 v[32:35], v[108:111], a[80:83], v[32:35]\n"
    "v_mfma_f32_16x16x32_bf16 v[36:39], v[112:115], a[80:83], v[36:39]\n"
    "v_mfma_f32_16x16x32_bf16 v[40:43], v[108:111], a[84:87], v[40:43]\n"
    "v_mfma_f32_16x16x32_bf16 v[44:47], v[112:115], a[84:87], v[44:47]\n"
    "s_bitcmp1_b32 s70, 11\n"
    "s_cbranch_scc0 LRAX%=\n"
    "v_mfma_f32_16x16x32_bf16 v[32:35], v[108:111], a[88:91], v[32:35]\n"
    "v_mfma_f32_16x16x32_bf16 v[36:39], v[112:115], a[88:91], v[36:39]\n"
    "v_mfma_f32_16x16x32_bf16 v[40:43], v[108:111], a[92:95], v[40:43]\n"
    "v_mfma_f32_16x16x32_bf16 v[44:47], v[112:115], a[92:95], v[44:47]\n"
    "s_bitcmp1_b32 s70, 12\n"
    "s_cbranch_scc0 LRAX%=\n"
    "v_mfma_f32_16x16x32_bf16 v[32:35], v[108:111], a[96:99], v[32:35]\n"
    "v_mfma_f32_16x16x32_bf16 v[36:39], v[112:115], a[96:99], v[36:39]\n"
    "v_mfma_f32_16x16x32_bf16 v[40:43], v[108:111], a[100:103], v[40:43]\n"
    "v_mfma_f32_16x16x32_bf16 v[44:47], v[112:115], a[100:103], v[44:47]\n"
    "s_bitcmp1_b32 s70, 13\n"
    "s_cbranch_scc0 LRAX%=\n"
    "v_mfma_f32_16x16x32_bf16 v[32:35], v[108:111], a[104:107], v[32:35]\n"
    "v_mfma_f32_16x16x32_bf16 v[36:39], v[112:115], a[104:107], v[36:39]\n"
    "v_mfma_f32_16x16x32_bf16 v[40:43], v[108:111], a[108:111], v[40:43]\n"
    "v_mfma_f32_16x16x32_bf16 v[44:47], v[112:115], a[108:111], v[44:47]\n"
    "s_bitcmp1_b32 s70, 14\n"
    "s_cbranch_scc0 LRAX%=\n"
    "v_mfma_f32_16x16x32_bf16 v[32:35], v[108:111], a[112:115], v[32:35]\n"
    "v_mfma_f32_16x16x32_bf16 v[36:39], v[112:115], a[112:115], v[36:39]\n"
    "v_mfma_f32_16x16x32_bf16 v[40:43], v[108:111], a[116:119], v[40:43]\n"
    "v_mfma_f32_16x16x32_bf16 v[44:47], v[112:115], a[116:119], v[44:47]\n"
    "s_bitcmp1_b32 s70, 15\n"
    "s_cbranch_scc0 LRAX%=\n"
    "v_mfma_f32_16x16x32_bf16 v[32:35], v[108:111], a[120:123], v[32:35]\n"
    "v_mfma_f32_16x16x32_bf16 v[36:39], v[112:115], a[120:123], v[36:39]\n"
    "v_mfma_f32_16x16x32_bf16 v[40:43], v[108:111], a[124:127], v[40:43]\n"
    "v_mfma_f32_16x16x32_bf16 v[44:47], v[112:115], a[124:127], v[44:47]\n"
    "s_branch LRDONE%=\n"
    "LRAX%=:\n"
    "s_cmp_eq_u32 s58, 0\n"
    "s_cbranch_scc1 LRNP%=\n"
    "s_sleep 6\n"
    "s_cmp_eq_u32 s58, 0xffff\n"
    "s_cbranch_scc0 LRPOLL%=\n"
    "s_sleep 16\n"
    "s_branch LRPOLL%=\n"
    "LRDONE%=:\n"
    "s_nop 7\n"
    "v_cvt_pk_bf16_f32 v64, v32, v33\n"
    "v_cvt_pk_bf16_f32 v65, v34, v35\n"
    "v_cvt_pk_bf16_f32 v66, v36, v37\n"
    "v_cvt_pk_bf16_f32 v67, v38, v39\n"
    "buffer_store_dwordx4 v[64:67], v119, s[52:55], s77 offen\n"
    "v_cvt_pk_bf16_f32 v68, v40, v41\n"
    "v_cvt_pk_bf16_f32 v69, v42, v43\n"
    "v_cvt_pk_bf16_f32 v70, v44, v45\n"
    "v_cvt_pk_bf16_f32 v71, v46, v47\n"
    "buffer_store_dwordx4 v[68:71], v120, s[52:55], s77 offen\n"
    "s_cmp_eq_u32 s77, 0\n"
    "s_cbranch_scc0 LEND%=\n"
    "s_mov_b32 s77, 1024\n"
    "s_mov_b32 s76, 16\n"
    "s_mov_b32 s71, 1310720\n"
    "s_branch LRINIT%=\n"
    "LEND%=:\n"
    :: "s"(__builtin_amdgcn_kernarg_segment_ptr()), "s"(blockIdx.x), "s"(w)
    : "v0", "v1", "v2", "v3", "v4", "v5", "v6", "v7", "v8", "v9", "v10", "v11", "v12", "v13", "v14", "v15", "v16", "v17", "v18", "v19", "v20", "v21", "v22", "v23", "v24", "v25", "v26", "v27", "v28", "v29", "v30", "v31", "v32", "v33", "v34", "v35", "v36", "v37", "v38", "v39", "v40", "v41", "v42", "v43", "v44", "v45", "v46", "v47", "v48", "v49", "v50", "v51", "v52", "v53", "v54", "v55", "v56", "v57", "v58", "v59", "v60", "v61", "v62", "v63", "v64", "v65", "v66", "v67", "v68", "v69", "v70", "v71", "v72", "v73", "v74", "v75", "v76", "v77", "v78", "v79", "v80", "v81", "v82", "v83", "v84", "v85", "v86", "v87", "v88", "v89", "v90", "v91", "v92", "v93", "v94", "v95", "v96", "v97", "v98", "v99", "v100", "v101", "v102", "v103", "v104", "v105", "v106", "v107", "v108", "v109", "v110", "v111", "v112", "v113", "v114", "v115", "v116", "v117", "v118", "v119", "v120", "v121", "v122", "v123", "v124", "v125", "v126", "v127", "v128", "v129", "v130", "v131", "v132", "v133", "v134", "v135", "v136", "v137", "v138", "v139", "v140", "v141", "v142", "v143", "v144", "v145", "v146", "v147", "v148", "v149", "v150", "v151", "v152", "v153", "v154", "v155", "v156", "v157", "v158", "v159", "v160", "v161", "v162", "v163", "v164", "v165", "v166", "v167", "v168", "v169", "v170", "v171", "v172", "v173", "v174", "v175", "v176", "v177", "v178", "v179", "v180", "v181", "v182", "v183", "v184", "v185", "v186", "v187", "v188", "v189", "v190", "v191", "v192", "v193", "v194", "v195", "v196", "v197", "v198", "v199", "v200", "v201", "v202", "v203", "v204", "v205", "v206", "v207", "v208", "v209", "v210", "v211", "v212", "v213", "v214", "v215", "v216", "v217", "v218", "v219", "v220", "v221", "v222", "v223", "v224", "v225", "v226", "v227", "v228", "v229", "v230", "v231", "v232", "v233", "v234", "v235", "v236", "v237", "v238", "v239", "v240", "v241", "v242", "v243", "v244", "v245", "v246", "v247", "v248", "v249", "v250", "v251", "v252", "v253", "v254", "v255", "a0", "a1", "a2", "a3", "a4", "a5", "a6", "a7", "a8", "a9", "a10", "a11", "a12", "a13", "a14", "a15", "a16", "a17", "a18", "a19", "a20", "a21", "a22", "a23", "a24", "a25", "a26", "a27", "a28", "a29", "a30", "a31", "a32", "a33", "a34", "a35", "a36", "a37", "a38", "a39", "a40", "a41", "a42", "a43", "a44", "a45", "a46", "a47", "a48", "a49", "a50", "a51", "a52", "a53", "a54", "a55", "a56", "a57", "a58", "a59", "a60", "a61", "a62", "a63", "a64", "a65", "a66", "a67", "a68", "a69", "a70", "a71", "a72", "a73", "a74", "a75", "a76", "a77", "a78", "a79", "a80", "a81", "a82", "a83", "a84", "a85", "a86", "a87", "a88", "a89", "a90", "a91", "a92", "a93", "a94", "a95", "a96", "a97", "a98", "a99", "a100", "a101", "a102", "a103", "a104", "a105", "a106", "a107", "a108", "a109", "a110", "a111", "a112", "a113", "a114", "a115", "a116", "a117", "a118", "a119", "a120", "a121", "a122", "a123", "a124", "a125", "a126", "a127", "a128", "a129", "a130", "a131", "a132", "a133", "a134", "a135", "a136", "a137", "a138", "a139", "a140", "a141", "a142", "a143", "a144", "a145", "a146", "a147", "a148", "a149", "a150", "a151", "a152", "a153", "a154", "a155", "a156", "a157", "a158", "a159", "a160", "a161", "a162", "a163", "a164", "a165", "a166", "a167", "a168", "a169", "a170", "a171", "a172", "a173", "a174", "a175", "a176", "a177", "a178", "a179", "a180", "a181", "a182", "a183", "a184", "a185", "a186", "a187", "a188", "a189", "a190", "a191", "a192", "a193", "a194", "a195", "a196", "a197", "a198", "a199", "a200", "a201", "a202", "a203", "a204", "a205", "a206", "a207", "a208", "a209", "a210", "a211", "a212", "a213", "a214", "a215", "a216", "a217", "a218", "a219", "a220", "a221", "a222", "a223", "a224", "a225", "a226", "a227", "a228", "a229", "a230", "a231", "a232", "a233", "a234", "a235", "a236", "a237", "a238", "a239", "a240", "a241", "a242", "a243", "a244", "a245", "a246", "a247", "a248", "a249", "a250", "a251", "a252", "a253", "a254", "a255", "s4", "s5", "s6", "s7", "s8", "s9", "s10", "s11", "s12", "s13", "s14", "s15", "s16", "s17", "s18", "s19", "s20", "s21", "s22", "s23", "s24", "s25", "s26", "s27", "s28", "s29", "s30", "s31", "s34", "s35", "s36", "s37", "s38", "s39", "s40", "s41", "s42", "s43", "s44", "s45", "s46", "s47", "s48", "s49", "s50", "s51", "s52", "s53", "s54", "s55", "s56", "s57", "s58", "s59", "s60", "s61", "s62", "s63", "s64", "s65", "s66", "s67", "s68", "s69", "s70", "s71", "s72", "s73", "s74", "s75", "s76", "s77", "s78", "s79", "s80", "s81", "s82", "s83", "s84", "s85", "s86", "s87", "s88", "s89", "s90", "s91", "s92", "s93", "s94", "s95", "s96", "s97", "s98", "s99", "vcc", "scc", "memory");
}

extern "C" void kernel_launch(void* const* d_in, const int* in_sizes, int n_in, void* d_out, int out_size, void* d_ws, size_t ws_size, hipStream_t stream) {
  Ptrs P;
  for (int br = 0; br < 2; ++br) {
    for (int i = 0; i < 5; ++i) P.p[br][i] = d_in[5 * br + i];
    P.p[br][5] = d_ws; P.p[br][6] = d_out; P.p[br][7] = d_in[9];
  }
  (void)hipFuncSetAttribute((const void*)fused_kernel, hipFuncAttributeMaxDynamicSharedMemorySize, 163840);
  fused_kernel<<<256, 256, 163840, stream>>>(P);
}
```
